```python
import torch

def _softmax(x, axis=-1):
    return torch.softmax(x, dim=axis)
def _gelu(x):
    return torch.nn.functional.gelu(x, approximate="tanh")
def _silu(x):
    return torch.nn.functional.silu(x)
def _sigmoid(x):
    return torch.sigmoid(x)
def _layernorm(x, axis=-1, eps=1e-5):
    m = x.mean(dim=axis, keepdim=True)
    v = x.var(dim=axis, keepdim=True, unbiased=False)
    return (x - m) / torch.sqrt(v + eps)
def _scatter_add(tgt, idx, src, axis=0):
    return tgt.clone().index_add_(axis, idx, src)
def _segment_sum(src, seg, n):
    out = torch.zeros((n,) + src.shape[1:], dtype=src.dtype)
    return out.index_add_(0, seg, src)
def _pad(x, pw):
    flat = tuple(v for pair in reversed(pw) for v in pair)
    return torch.nn.functional.pad(x, flat)

kernel_name = 'recurrent_scan__ssm_conv_s5__mixer_token_mlp__prompt'

def setup_inputs():

    _w = (lambda a: (lambda t: t.to(torch.bfloat16) if t.is_floating_point() else t)(torch.as_tensor(a)))
    def _A_setup():
        B, S, D = 1, 2048, 512
        P = D
        Lambda_re = -0.5 * (lambda _x: torch.exp(torch.as_tensor(_x)))((lambda *s: torch.randn(*s, dtype=torch.bfloat16) * 0.4)(P))
        Lambda_im = (lambda *s: torch.randn(*s, dtype=torch.bfloat16) * 0.8)(P) * 1.0
        B_re = (lambda *s: torch.randn(*s, dtype=torch.bfloat16) * 0.4)(P, D) * (D ** -0.5)
        B_im = (lambda *s: torch.randn(*s, dtype=torch.bfloat16) * 0.4)(P, D) * (D ** -0.5)
        C_re = (lambda *s: torch.randn(*s, dtype=torch.bfloat16) * 0.4)(D, P) * (P ** -0.5)
        C_im = (lambda *s: torch.randn(*s, dtype=torch.bfloat16) * 0.4)(D, P) * (P ** -0.5)
        log_dt = (lambda *s: torch.randn(*s, dtype=torch.bfloat16) * 0.4)(P) * 0.3 - 2.0
        Dskip = (lambda *s: torch.randn(*s, dtype=torch.bfloat16) * 0.2)(D)
        return {
            "x": _w((lambda *s: torch.randn(*s, dtype=torch.bfloat16) * 0.05)(B, S, D)),
            "Lambda_re": _w(Lambda_re),
            "Lambda_im": _w(Lambda_im),
            "B_re": _w(B_re),
            "B_im": _w(B_im),
            "C_re": _w(C_re),
            "C_im": _w(C_im),
            "log_dt": _w(log_dt),
            "Dskip": _w(Dskip),
        }
    _A_in = _A_setup()

    def _B_setup():
        B, S, D, F = 1, 2048, 512, 1376
        return {
            "x": _w((lambda *s: torch.randn(*s, dtype=torch.bfloat16) * 0.05)(B, S, D)),
            "w1": _w((lambda *s: torch.randn(*s, dtype=torch.bfloat16) * 0.4)(S, F) * (S ** -0.5)),
            "b1": _w((lambda *s: torch.randn(*s, dtype=torch.bfloat16) * 3.2)(F) * 0.02),
            "w2": _w((lambda *s: torch.randn(*s, dtype=torch.bfloat16) * 0.8)(F, S) * (F ** -0.5)),
            "b2": _w((lambda *s: torch.randn(*s, dtype=torch.bfloat16) * 1.6)(S) * 0.02),
        }
    _B_in = _B_setup()

    return {
        "A_x": _A_in["x"],
        "A_Lambda_re": _A_in["Lambda_re"],
        "A_Lambda_im": _A_in["Lambda_im"],
        "A_B_re": _A_in["B_re"],
        "A_B_im": _A_in["B_im"],
        "A_C_re": _A_in["C_re"],
        "A_C_im": _A_in["C_im"],
        "A_log_dt": _A_in["log_dt"],
        "A_Dskip": _A_in["Dskip"],
        "B_x": _B_in["x"],
        "B_w1": _B_in["w1"],
        "B_b1": _B_in["b1"],
        "B_w2": _B_in["w2"],
        "B_b2": _B_in["b2"],
    }

def reference(A_x, A_Lambda_re, A_Lambda_im, A_B_re, A_B_im, A_C_re, A_C_im, A_log_dt, A_Dskip, B_x, B_w1, B_b1, B_w2, B_b2):
    def _A_ref(x, Lambda_re, Lambda_im, B_re, B_im, C_re, C_im, log_dt, Dskip):
        Bsz, L, H = x.shape
        Lam = Lambda_re + 1j * Lambda_im
        Bmat = B_re + 1j * B_im
        Cmat = C_re + 1j * C_im
        dt = torch.exp(log_dt)
        Lam_bar = torch.exp(dt * Lam)
        B_bar = ((Lam_bar - 1.0) / Lam)[:, None] * Bmat
        Bu = torch.einsum("bld,pd->blp", x + 0j, B_bar)
        h = torch.zeros_like(Bu[:, 0, :])
        hs = []
        for t in range(L):
            h = Lam_bar[None, :] * h + Bu[:, t, :]
            hs.append(h)
        hseq = torch.stack(hs, 1)
        y = 2.0 * torch.einsum("blp,dp->bld", hseq, Cmat).real + x * Dskip[None, None, :]
        return y

    def _B_ref(x, w1, b1, w2, b2):
        B, S, D = x.shape
        h = _layernorm(x, axis=-1)
        u = torch.einsum("bsd,sf->bdf", h, w1) + b1
        u = _gelu(u)
        y = torch.einsum("bdf,fs->bds", u, w2) + b2
        return (lambda x, *d: x.permute(*d))(y, 0, 2, 1)

    x = A_x
    xn = _layernorm(x, axis=-1)
    s = _A_ref(xn, A_Lambda_re, A_Lambda_im, A_B_re, A_B_im, A_C_re, A_C_im, A_log_dt, A_Dskip)
    g = _silu(_B_ref(xn, B_w1, B_b1, B_w2, B_b2))
    return x + s * g
```

```cpp
#include <hip/hip_runtime.h>
#include <hip/hip_bf16.h>
#include <stdint.h>
typedef unsigned short u16;
constexpr int S=2048, D=512, P=512, F=1376;
__device__ __forceinline__ float bf2f(u16 v){ return __uint_as_float(((unsigned)v)<<16); }
__device__ __forceinline__ u16 f2bf(float f){ unsigned u=__float_as_uint(f); return (u16)((u + 0x7FFF + ((u>>16)&1))>>16); }

__global__ void ln_k(const u16* x, float* xn, float* hn){
  int s=blockIdx.x, l=threadIdx.x;
  float v[8]; float sum=0;
  for(int j=0;j<8;j++){ v[j]=bf2f(x[s*D+l*8+j]); sum+=v[j]; }
  for(int o=32;o;o>>=1) sum+=__shfl_xor(sum,o);
  float m=sum/D; float q=0;
  for(int j=0;j<8;j++){ v[j]-=m; q+=v[j]*v[j]; }
  for(int o=32;o;o>>=1) q+=__shfl_xor(q,o);
  float r=rsqrtf(q/D+1e-5f);
  sum=0;
  for(int j=0;j<8;j++){ v[j]=bf2f(f2bf(v[j]*r)); xn[s*D+l*8+j]=v[j]; sum+=v[j]; }
  for(int o=32;o;o>>=1) sum+=__shfl_xor(sum,o);
  m=sum/D; q=0;
  for(int j=0;j<8;j++){ v[j]-=m; q+=v[j]*v[j]; }
  for(int o=32;o;o>>=1) q+=__shfl_xor(q,o);
  r=rsqrtf(q/D+1e-5f);
  for(int j=0;j<8;j++){ hn[s*D+l*8+j]=bf2f(f2bf(v[j]*r)); }
}
__global__ void g1_k(const float* xn, const u16* Bre, const u16* Bim, float* Pr, float* Pi){
  int s=blockIdx.x, p=threadIdx.x;
  float a=0,b=0;
  for(int d=0;d<D;d++){ float xv=xn[s*D+d]; a+=xv*bf2f(Bre[p*D+d]); b+=xv*bf2f(Bim[p*D+d]); }
  Pr[s*P+p]=a; Pi[s*P+p]=b;
}
__global__ void scan_k(const u16* Lre,const u16* Lim,const u16* ldt, float* Pr, float* Pi){
  int p=blockIdx.x*64+threadIdx.x;
  float lr=bf2f(Lre[p]), li=bf2f(Lim[p]); float dt=expf(bf2f(ldt[p]));
  float e=expf(dt*lr); float ar=e*cosf(dt*li), ai=e*sinf(dt*li);
  float nr=ar-1.f, ni=ai; float den=lr*lr+li*li;
  float cr=(nr*lr+ni*li)/den, ci=(ni*lr-nr*li)/den;
  float hr=0,hi=0;
  for(int s=0;s<S;s++){
    float a=Pr[s*P+p], b=Pi[s*P+p];
    float br=cr*a-ci*b, bi=cr*b+ci*a;
    float nhr=ar*hr-ai*hi+br, nhi=ar*hi+ai*hr+bi;
    hr=nhr;hi=nhi; Pr[s*P+p]=bf2f(f2bf(hr)); Pi[s*P+p]=bf2f(f2bf(hi));
  }
}
__global__ void g3_k(const float* hn, const u16* w1, const u16* b1, float* u){
  int d=blockIdx.x; int f=blockIdx.y*256+threadIdx.x; if(f>=F) return;
  float a=0;
  for(int s=0;s<S;s++) a+=hn[s*D+d]*bf2f(w1[s*F+f]);
  a+=bf2f(b1[f]);
  float t=tanhf(0.7978845608028654f*(a+0.044715f*a*a*a));
  u[d*F+f]=bf2f(f2bf(0.5f*a*(1.f+t)));
}
__global__ void fin_k(const u16* x,const float* xn,const float* Hr,const float* Hi,const u16* Cre,const u16* Cim,const u16* Dsk,const float* u,const u16* w2,const u16* b2,float* out){
  int s=blockIdx.x, d=threadIdx.x;
  float y=0;
  for(int p=0;p<P;p++) y+=Hr[s*P+p]*bf2f(Cre[d*P+p])-Hi[s*P+p]*bf2f(Cim[d*P+p]);
  y=2.f*y+xn[s*D+d]*bf2f(Dsk[d]);
  float g=0;
  for(int f=0;f<F;f++) g+=u[d*F+f]*bf2f(w2[f*S+s]);
  g+=bf2f(b2[s]);
  g=g/(1.f+expf(-g));
  out[s*D+d]=bf2f(x[s*D+d])+y*g;
}
extern "C" void kernel_launch(void* const* d_in, const int* in_sizes, int n_in, void* d_out, int out_size, void* d_ws, size_t ws_size, hipStream_t stream){
  const u16* x=(const u16*)d_in[0]; const u16* Lre=(const u16*)d_in[1]; const u16* Lim=(const u16*)d_in[2];
  const u16* Bre=(const u16*)d_in[3]; const u16* Bim=(const u16*)d_in[4]; const u16* Cre=(const u16*)d_in[5]; const u16* Cim=(const u16*)d_in[6];
  const u16* ldt=(const u16*)d_in[7]; const u16* Dsk=(const u16*)d_in[8];
  const u16* w1=(const u16*)d_in[10]; const u16* b1=(const u16*)d_in[11]; const u16* w2=(const u16*)d_in[12]; const u16* b2=(const u16*)d_in[13];
  float* ws=(float*)d_ws;
  float* xn=ws; float* hn=xn+S*D; float* Pr=hn+S*D; float* Pi=Pr+S*P; float* u=Pi+S*P;
  ln_k<<<S,64,0,stream>>>(x,xn,hn);
  g1_k<<<S,512,0,stream>>>(xn,Bre,Bim,Pr,Pi);
  scan_k<<<8,64,0,stream>>>(Lre,Lim,ldt,Pr,Pi);
  g3_k<<<dim3(D,6),256,0,stream>>>(hn,w1,b1,u);
  fin_k<<<S,512,0,stream>>>(x,xn,Pr,Pi,Cre,Cim,Dsk,u,w2,b2,(float*)d_out);
}
```

```cpp
#if defined(__HIP_DEVICE_COMPILE__) && __HIP_DEVICE_COMPILE__
#pragma clang attribute push(__attribute__((target("no-packed-fp32-ops"))), apply_to = function)
#endif
#include <hip/hip_runtime.h>
#include <stdint.h>

typedef unsigned short u16;
typedef unsigned int u32;
typedef unsigned long long u64;
typedef __attribute__((ext_vector_type(2))) unsigned u32x2;
typedef __attribute__((ext_vector_type(4))) unsigned u32x4;
typedef __attribute__((ext_vector_type(2))) float f32x2;
typedef __attribute__((ext_vector_type(4))) float f32x4;
typedef __attribute__((ext_vector_type(16))) float f32x16;
typedef __attribute__((ext_vector_type(2))) __bf16 bf16x2;
typedef __attribute__((ext_vector_type(4))) __bf16 bf16x4;
typedef __attribute__((ext_vector_type(8))) __bf16 bf16x8;
#define LAS __attribute__((address_space(3)))
#define LP(T, a) ((LAS T*)(a))
#define VLP(T, a) ((volatile LAS T*)(a))
#define DEV __device__ __forceinline__
#define SB() __builtin_amdgcn_sched_barrier(0)

#define DIAG 0
#define D1 12
#define D2 6
#define DH 24
#define D3 12
#define PSLEEP 8
#define ROT1 (idx & 7)

constexpr int S = 2048, D = 512, P = 512, F = 1376;
constexpr int NWAVE = 12;
constexpr u32 OFF_E = 0, OFF_STAT = 32768, OFF_AGG = 65536, OFF_FA = 196608, OFF_FH = 262144, OFF_FU = 294912;
constexpr u32 OFF_HN = 524288;
constexpr u32 OFF_HX = OFF_HN + (2u << 20);
constexpr u32 OFF_U = OFF_HX + (4u << 20);
constexpr u32 WS_BYTES = OFF_U + 8u * 176u * 1024u;
constexpr u32 OFF_DIAG = 16u << 20;
constexpr int R = 5;
constexpr u32 L_DUMP = 4 * R * 4096, L_LNT = L_DUMP + 65536, L_SEG = L_LNT, L_CIN = L_LNT + 4096, L_PW = L_LNT + 8704, L_ST1 = L_PW + 4096,
              L_ST2 = L_ST1 + 1024, L_CC = L_ST2 + 1024, L_H = L_CC + 512, L_BS = L_H + 256, L_ST0 = L_BS + 256, L_SYNC = L_ST0 + 64,
              LDS_BYTES = L_SYNC + 256;
enum { Y_LN = 0, Y_A, Y_B, Y_BS, Y_G1, Y_D1, Y_ST, Y_E, Y_CIN, Y_HS, Y_H, Y_D3, Y_R3, Y_U, Y_D2, Y_R2, Y_D4, Y_GATE1, Y_GATE2, Y_GATE3 };
#define YA(i) (L_SYNC + 4 * (i))
constexpr int SC1 = 16;

struct Params {
  const u16 *x, *Lre, *Lim, *Bre, *Bim, *Cre, *Cim, *ldt, *Dsk, *w1, *b1, *w2, *b2;
  float* out;
  char* ws;
};

DEV float bf2f(u32 v) { return __uint_as_float(v << 16); }
DEV float bfhi(u32 v) { return __uint_as_float(v & 0xffff0000u); }
DEV u32 pk2(float a, float b) { f32x2 t = {a, b}; bf16x2 r = __builtin_convertvector(t, bf16x2); return __builtin_bit_cast(u32, r); }
DEV int uni(int v) { return __builtin_amdgcn_readfirstlane(v); }
DEV void cfence() { asm volatile("" ::: "memory"); }
template <int CTRL, int RM> DEV float dpp_add(float x) {
  return x + __builtin_bit_cast(float, __builtin_amdgcn_update_dpp(0, __builtin_bit_cast(int, x), CTRL, RM, 0xf, false));
}
DEV void wave_sum2(float& x, float& y) {
  x = dpp_add<0xB1, 0xf>(x); y = dpp_add<0xB1, 0xf>(y); x = dpp_add<0x4E, 0xf>(x); y = dpp_add<0x4E, 0xf>(y);
  x = dpp_add<0x141, 0xf>(x); y = dpp_add<0x141, 0xf>(y); x = dpp_add<0x140, 0xf>(x); y = dpp_add<0x140, 0xf>(y);
  x = dpp_add<0x142, 0xa>(x); y = dpp_add<0x142, 0xa>(y); x = dpp_add<0x143, 0xc>(x); y = dpp_add<0x143, 0xc>(y);
  x = __builtin_bit_cast(float, __builtin_amdgcn_readlane(__builtin_bit_cast(int, x), 63));
  y = __builtin_bit_cast(float, __builtin_amdgcn_readlane(__builtin_bit_cast(int, y), 63));
}
DEV u32x4 make_srd(const void* p, u32 bytes) {
  u64 a = (u64)p; u32x4 r = {(u32)a, (u32)(a >> 32) & 0xffffu, bytes, 0x00020000u}; return r;
}
typedef __amdgpu_buffer_rsrc_t rsrc_t;
DEV rsrc_t make_rsrc(const void* p, u32 bytes) { return __builtin_amdgcn_make_buffer_rsrc((void*)p, 0, bytes, 0x00020000); }

template <int SL = 1> DEV void lds_wait(int y, int target, int& budget) {
  u32 t;
  budget = uni(budget);
  asm volatile(
      "1:\n\t"
      "ds_read_b32 %[t], %[a]\n\t"
      "s_sub_i32 %[bud], %[bud], 1\n\t"
      "s_cmp_lt_i32 %[bud], 0\n\t"
      "s_waitcnt lgkmcnt(0)\n\t"
      "v_cmp_le_i32 vcc, %[tg], %[t]\n\t"
      "s_cbranch_vccnz 2f\n\t"
      "s_sleep %[sl]\n\t"
      "s_cbranch_scc0 1b\n"
      "2:"
      : [t] "=&v"(t), [bud] "+s"(budget) : [a] "v"(YA(y)), [tg] "s"(target), [sl] "n"(SL) : "vcc", "scc", "memory");
}
DEV void lds_inc(int y) {
  asm volatile("s_mov_b64 exec, 1\n\tds_add_u32 %0, %1\n\ts_mov_b64 exec, -1" :: "v"(YA(y)), "v"(1) : "memory");
}
DEV int lds_inc_rtn(int y) {
  int old;
  asm volatile("s_mov_b64 exec, 1\n\tds_add_rtn_u32 %0, %1, %2\n\ts_mov_b64 exec, -1\n\ts_waitcnt lgkmcnt(0)" : "=&v"(old) : "v"(YA(y)), "v"(1) : "memory");
  return uni(old);
}
DEV void drain() { asm volatile("s_waitcnt vmcnt(0)" ::: "memory"); }
DEV bf16x8 tr_pair(u32 a0, u32 a1) {
  bf16x4 lo = __builtin_amdgcn_ds_read_tr16_b64_v4bf16(LP(bf16x4, a0));
  bf16x4 hi = __builtin_amdgcn_ds_read_tr16_b64_v4bf16(LP(bf16x4, a1));
  return __builtin_shufflevector(lo, hi, 0, 1, 2, 3, 4, 5, 6, 7);
}
#define STAMP(i) do {} while (0)
#define DUMPT() do {} while (0)

template <int O> DEV void dma_m(u32x4 srd, u32 v, u32 so, u32 m0v) {
  asm volatile("s_mov_b32 m0, %3\n\ts_nop 0\n\tbuffer_load_dwordx4 %0, %1, %2 offen offset:%4 lds" :: "v"(v), "s"(srd), "s"(so), "s"(m0v), "n"(O) : "memory");
}
template <int O> DEV void dma_n(u32x4 srd, u32 v, u32 so) {
  asm volatile("buffer_load_dwordx4 %0, %1, %2 offen offset:%3 lds" :: "v"(v), "s"(srd), "s"(so), "n"(O) : "memory");
}
template <int O> DEV void xload(u32x4& d, u32x4 srd, u32 v, u32 so) {
  asm volatile("buffer_load_dwordx4 %0, %1, %2, %3 offen offset:%4 sc1" : "=v"(d) : "v"(v), "s"(srd), "s"(so), "n"(O) : "memory");
}
template <int N> DEV void xwait(u32x4& a, u32x4& b) { asm volatile("s_waitcnt vmcnt(%2)" : "+v"(a), "+v"(b) : "n"(N) : "memory"); }
#define MFMA32(c, a, b) c = __builtin_amdgcn_mfma_f32_32x32x16_bf16(a, b, c, 0, 0, 0)
DEV bf16x8 asbf(u32x4 v) { return __builtin_bit_cast(bf16x8, v); }

struct Eng {
  u32x4 wsrd, xsrd;
  u32 wv[4], wso, wstep;
  u32 wdst, rsl, rlo, rhi;
  u32 xv, xso, xs1, xs2;
  u32 aW, aC;
  u32x4 x[4][2];
  bf16x8 a[2][2];
  f32x16 c[2][2];
};
DEV u32 nxt(const Eng& e, u32 s) { s += 4096; return s == e.rhi ? e.rlo : s; }
DEV void w_slot(Eng& e) {
  dma_m<0>(e.wsrd, e.wv[0], e.wso, e.wdst); dma_n<1024>(e.wsrd, e.wv[1], e.wso); dma_n<2048>(e.wsrd, e.wv[2], e.wso); dma_n<3072>(e.wsrd, e.wv[3], e.wso);
  e.wso += e.wstep; e.wdst = nxt(e, e.wdst);
}
DEV void w_set(Eng& e, u32x4 srd, u32 v0, u32 v1, u32 pitch8, u32 so, u32 step) {
  e.wsrd = srd; e.wv[0] = v0; e.wv[1] = v1 + pitch8 - 1024; e.wv[2] = v0 + 2 * pitch8 - 2048; e.wv[3] = v1 + 3 * pitch8 - 3072; e.wso = so; e.wstep = step;
}
template <bool TR, int U> DEV void frag_read(Eng& e) {
  if (TR) {
    const u32 b = e.rsl + (U & 1) * 2048;
    e.a[U & 1][0] = tr_pair(b + e.aW, b + e.aW + 512);
    e.a[U & 1][1] = tr_pair(b + (e.aW ^ 64), b + (e.aW ^ 64) + 512);
  } else {
    e.a[U & 1][0] = *LP(bf16x8, e.rsl + (e.aC ^ (U << 5)));
    e.a[U & 1][1] = *LP(bf16x8, nxt(e, e.rsl) + (e.aC ^ (U << 5)));
  }
}
template <bool TR, int U, bool DOW, bool DOX, int VM, bool NEXT>
DEV void unit(Eng& e) {
  xwait<VM>(e.x[U][0], e.x[U][1]);
  if (TR) { if (U & 1) e.rsl = nxt(e, e.rsl); }
  else if (U == 3) e.rsl = nxt(e, nxt(e, e.rsl));
  if (NEXT) frag_read<TR, (U + 1) & 3>(e);
  SB();
  constexpr bool LATE = !TR && U == 2;
  const bf16x8 f0 = e.a[U & 1][0], f1 = e.a[U & 1][1], x0 = asbf(e.x[U][0]), x1 = asbf(e.x[U][1]);
  if (TR) MFMA32(e.c[0][0], f0, x0); else MFMA32(e.c[0][0], x0, f0);
  SB();
  if (DOW && !LATE) { if (U & 1) dma_n<2048>(e.wsrd, e.wv[2], e.wso); else dma_m<0>(e.wsrd, e.wv[0], e.wso, e.wdst); }
  SB();
  if (TR) MFMA32(e.c[1][0], f1, x0); else MFMA32(e.c[0][1], x0, f1);
  SB();
  if (DOX) xload<(U & 1) * 2048>(e.x[U][0], e.xsrd, e.xv, e.xso);
  SB();
  if (TR) MFMA32(e.c[0][1], f0, x1); else MFMA32(e.c[1][0], x1, f0);
  SB();
  if (DOW && LATE) { asm volatile("s_waitcnt lgkmcnt(0)" ::: "memory"); dma_m<0>(e.wsrd, e.wv[0], e.wso, e.wdst); }
  if (DOW && !LATE) { if (U & 1) dma_n<3072>(e.wsrd, e.wv[3], e.wso); else dma_n<1024>(e.wsrd, e.wv[1], e.wso); }
  SB();
  MFMA32(e.c[1][1], TR ? f1 : x1, TR ? x1 : f1);
  SB();
  if (DOX) xload<(U & 1) * 2048 + 512>(e.x[U][1], e.xsrd, e.xv, e.xso);
  if (DOW && LATE) dma_n<1024>(e.wsrd, e.wv[1], e.wso);
  if (U & 1) {
    if (DOX) e.xso += U == 1 ? e.xs1 : e.xs2;
    if (DOW) { e.wso += e.wstep; e.wdst = nxt(e, e.wdst); }
  }
  SB();
}
template <bool TR, bool NEXT> DEV void group(Eng& e) {
  unit<TR, 0, true, true, 12, true>(e); unit<TR, 1, true, true, 12, true>(e); unit<TR, 2, true, true, 12, true>(e); unit<TR, 3, true, true, 12, NEXT>(e);
}
DEV void x_prologue(Eng& e) {
  xload<0>(e.x[0][0], e.xsrd, e.xv, e.xso); xload<512>(e.x[0][1], e.xsrd, e.xv, e.xso);
  xload<2048>(e.x[1][0], e.xsrd, e.xv, e.xso); xload<2560>(e.x[1][1], e.xsrd, e.xv, e.xso);
  e.xso += e.xs1;
  xload<0>(e.x[2][0], e.xsrd, e.xv, e.xso); xload<512>(e.x[2][1], e.xsrd, e.xv, e.xso);
  xload<2048>(e.x[3][0], e.xsrd, e.xv, e.xso); xload<2560>(e.x[3][1], e.xsrd, e.xv, e.xso);
  e.xso += e.xs2;
  xwait<0>(e.x[0][0], e.x[0][1]);
}
DEV void dump16(f32x16& c, u32 a) {
#pragma unroll
  for (int g = 0; g < 4; g++) *LP(f32x4, a + g * 1024) = f32x4{c[4 * g], c[4 * g + 1], c[4 * g + 2], c[4 * g + 3]};
  c = f32x16{};
}
DEV void dump_all(Eng& e, int w, int lane) {
  const u32 a = L_DUMP + w * 16384 + lane * 16;
  dump16(e.c[0][0], a); dump16(e.c[0][1], a + 4096); dump16(e.c[1][0], a + 8192); dump16(e.c[1][1], a + 12288);
}
DEV void gather(float (&o)[8], int j, int lane) {
  const u32 a = L_DUMP + (j >> 1) * 4096 + (j & 1) * 2048 + lane * 16;
  f32x4 t[4][2];
#pragma unroll
  for (int e = 0; e < 4; e++) { t[e][0] = *LP(f32x4, a + e * 16384); t[e][1] = *LP(f32x4, a + e * 16384 + 1024); }
#pragma unroll
  for (int i = 0; i < 8; i++) o[i] = (t[0][i >> 2][i & 3] + t[1][i >> 2][i & 3]) + (t[2][i >> 2][i & 3] + t[3][i >> 2][i & 3]);
}
DEV void chan_const(u32 lre, u32 lim, u32 ldt, float& ar, float& ai, float& cr, float& ci) {
  const float lr = bf2f(lre), li = bf2f(lim), dt = __builtin_amdgcn_exp2f(1.4426950409f * bf2f(ldt));
  const float xr = dt * lr, rev = dt * li * 0.15915494309f;
  const float e = __builtin_amdgcn_exp2f(1.4426950409f * xr), em1 = expm1f(xr);
  const float fr = rev - rintf(rev);
  const float sn = __builtin_amdgcn_sinf(fr), cs = __builtin_amdgcn_cosf(fr), sh = __builtin_amdgcn_sinf(0.5f * fr);
  ar = e * cs; ai = e * sn;
  const float nr = em1 * cs - 2.f * sh * sh, ni = ai, den = 2.f / (lr * lr + li * li);
  cr = (nr * lr + ni * li) * den; ci = (ni * lr - nr * li) * den;
}
DEV void csq(float& r, float& i) { float t = r * r - i * i; i = 2.f * r * i; r = t; }

__global__ __launch_bounds__(NWAVE * 64) void s5_mixer_kernel(Params p) {
  const int tid = __builtin_amdgcn_workitem_id_x(), lane = tid & 63, w = uni(tid >> 6), b = __builtin_amdgcn_workgroup_id_x();
  const int xcd = b & 7, idx = b >> 3;
  const int koct = xcd * 32 + idx;
  const int c1 = 4 * (xcd >> 1) + (idx >> 3), g1 = 8 * (xcd & 1) + (idx & 7);
  const bool act3 = idx < 22;
  const int t3 = (xcd >> 1) * 22 + idx, ft = t3 >> 2, dt3 = 4 * (xcd & 1) + (t3 & 3);
  const int st = 8 * (xcd >> 1) + (idx >> 2), dt = 4 * (xcd & 1) + (idx & 3);
  const int hh = lane >> 5, l31 = lane & 31;
  int budget = 1 << 20;
  STAMP(0);

  if (w < 4) {
    Eng e;
    const u32 rw = lane >> 3, pos = lane & 7;
    const u32 swzR = (pos ^ (lane >> 4)) << 4, swzW = (pos ^ (((lane >> 4) & 1) << 2)) << 4;
    e.rlo = w * R * 4096; e.rhi = e.rlo + R * 4096; e.wdst = e.rlo; e.rsl = e.rlo;
    const u32 rot = ROT1;
    if (w == 0) *VLP(int, L_SYNC + lane * 4) = 0;
    asm volatile("s_waitcnt lgkmcnt(0)\n\ts_barrier" ::: "memory");
    {
      const u32x4 sb = make_srd(w < 2 ? p.Bre : p.Bim, P * D * 2);
      const u32 v0 = rw * 1024 + swzR, v1 = (v0 ^ 64) + 7168, base = (32 * g1 + 16 * (w & 1)) * 1024;
#pragma unroll 1
      for (int k = 0; k < 8; k++) {
        const u32 so = base + ((k + rot) & 7) * 128;
        dma_m<0>(sb, v0, so, L_DUMP + k * 8192 + w * 2048);
        dma_n<1024>(sb, v1, so);
      }
    }
    const u32 xbase = (128 * c1 + 32 * w) * 1024;
    { const u32 v0 = (16 * hh + (rw & 3)) * 1024 + swzR; w_set(e, make_srd(p.x, S * D * 2), v0, v0 ^ 64, 4096, xbase + rot * 128, 128); }
    auto x_wrap = [&]() { if (e.wso == xbase + 1024) e.wso = xbase; };
#pragma unroll 1
    for (int k = 0; k < 4; k++) { w_slot(e); x_wrap(); }
    STAMP(1);
    e.xsrd = make_srd(p.ws, WS_BYTES);
    e.aW = (8 * hh + ((lane & 15) >> 2)) * 128 + (((2 * (lane & 1) + ((lane >> 4) & 1)) ^ (((lane >> 3) & 1) << 2)) << 4) + ((lane >> 1) & 1) * 8;
    e.aC = l31 * 128 + ((hh ^ ((l31 >> 1) & 7)) << 4);
    auto set_w1 = [&]() { const u32 v = rw * 2752 + (ft == 21 ? (swzW & 48) : swzW); w_set(e, make_srd(p.w1, S * F * 2), v, v, 8 * 2752, w * 32 * 2752 + ft * 128, 128 * 2752); };
    auto set_w2 = [&]() { const u32 v = rw * 4096 + swzW; w_set(e, make_srd(p.w2, F * S * 2), v, v, 32768, w * 32 * 4096 + st * 128, 128 * 4096); };
    const u32 cbase = dt * 65536 + (w & 1) * 512;
    auto set_c = [&]() { const u32 v = rw * 1024 + swzR; w_set(e, make_srd(w < 2 ? p.Cre : p.Cim, D * P * 2), v, v ^ 64, 8192, cbase, 32768); };
    auto c_wrap = [&]() { if (e.wso >= cbase + 65536) e.wso += 128 - 65536; };
    asm volatile("s_waitcnt vmcnt(16)" ::: "memory");
    lds_inc(Y_B);
    e.c[0][0] = f32x16{}; e.c[0][1] = f32x16{}; e.c[1][0] = f32x16{}; e.c[1][1] = f32x16{};
    lds_wait<0>(Y_B, 4, budget);
    STAMP(2);
    {
      bf16x8 fa[2], fb[2][2];
      auto rd = [&](int par, u32 slot, u32 bp, int s) {
        fa[par] = *LP(bf16x8, slot + (e.aC ^ (s << 5)));
        fb[par][0] = *LP(bf16x8, bp + (e.aC ^ (s << 5)));
        fb[par][1] = *LP(bf16x8, bp + 4096 + (e.aC ^ (s << 5)));
      };
      asm volatile("s_waitcnt vmcnt(12)" ::: "memory");
      u32 bp = L_DUMP;
      rd(0, e.rsl, bp, 0);
#pragma unroll 1
      for (int k = 0; k < 8; k++) {
        if (k == 4) { if (act3) set_w1(); else set_c(); }
        rd(1, e.rsl, bp, 1); SB();
        MFMA32(e.c[0][0], fa[0], fb[0][0]); SB(); dma_m<0>(e.wsrd, e.wv[0], e.wso, e.wdst); SB(); MFMA32(e.c[0][1], fa[0], fb[0][1]); SB();
        rd(0, e.rsl, bp, 2); SB();
        MFMA32(e.c[0][0], fa[1], fb[1][0]); SB(); dma_n<1024>(e.wsrd, e.wv[1], e.wso); SB(); MFMA32(e.c[0][1], fa[1], fb[1][1]); SB();
        rd(1, e.rsl, bp, 3); SB();
        MFMA32(e.c[0][0], fa[0], fb[0][0]); SB(); dma_n<2048>(e.wsrd, e.wv[2], e.wso); SB(); MFMA32(e.c[0][1], fa[0], fb[0][1]); SB();
        e.rsl = nxt(e, e.rsl); bp += 8192;
        if (k < 7) { asm volatile("s_waitcnt vmcnt(11)" ::: "memory"); rd(0, e.rsl, bp, 0); }
        SB();
        MFMA32(e.c[0][0], fa[1], fb[1][0]); SB(); dma_n<3072>(e.wsrd, e.wv[3], e.wso); SB(); MFMA32(e.c[0][1], fa[1], fb[1][1]); SB();
        e.wso += e.wstep; e.wdst = nxt(e, e.wdst);
        if (k < 4) x_wrap(); else if (!act3) c_wrap();
      }
      STAMP(3);
      lds_inc(Y_G1);
      lds_wait<0>(Y_G1, 12, budget);
      const u32 a = L_DUMP + w * 8192 + lane * 16;
      dump16(e.c[0][0], a); dump16(e.c[0][1], a + 4096);
      lds_inc(Y_D1);
      STAMP(4);
    }
    auto set_hx = [&]() { e.xv = hh * 1024 + (16 * ((l31 >> 2) & 1) + 4 * (l31 >> 3) + (l31 & 3)) * 16; e.xso = OFF_HX + st * 131072 + w * 32768; e.xs1 = 4096; e.xs2 = 4096; };
    if (act3) {
      e.xv = hh * 1024 + l31 * 16; e.xso = OFF_HN + dt3 * 262144 + w * 4096; e.xs1 = 16384; e.xs2 = 16384;
      lds_wait(Y_GATE1, 1, budget);
      STAMP(5);
      x_prologue(e);
      frag_read<true, 0>(e);
#pragma unroll 1
      for (int g = 0; g < 7; g++) {
        if (g == 6) set_c();
        group<true, true>(e);
      }
      c_wrap();
      lds_wait(Y_GATE2, 1, budget);
      STAMP(6);
      set_hx();
      group<true, false>(e);
      c_wrap();
      STAMP(7);
      lds_wait(Y_HS, 8, budget);
      dump_all(e, w, lane);
      lds_inc(Y_D3);
      STAMP(8);
      xwait<12>(e.x[0][0], e.x[0][1]);
    } else {
      lds_wait<4>(Y_GATE2, 1, budget);
      set_hx();
      x_prologue(e);
    }
    frag_read<false, 0>(e);
#pragma unroll 1
    for (int g = 0; g < 3; g++) {
      if (g == 2) set_w2();
      group<false, true>(e);
      if (g < 2) c_wrap();
    }
    STAMP(9);
    lds_wait(Y_GATE3, 1, budget);
    STAMP(10);
    e.xv = hh * 1024 + l31 * 16; e.xso = OFF_U + dt * 180224 + w * 4096; e.xs1 = 16384; e.xs2 = 16384;
    group<false, false>(e);
    lds_wait(act3 ? Y_R3 : Y_HS, 8, budget);
    dump_all(e, w, lane);
    lds_inc(Y_D2);
    STAMP(11);
    xwait<12>(e.x[0][0], e.x[0][1]);
    frag_read<true, 0>(e);
#pragma unroll 1
    for (int g = 0; g < 3; g++) group<true, true>(e);
    unit<true, 0, true, true, 12, true>(e); unit<true, 1, true, true, 12, true>(e);
    unit<true, 2, false, true, 12, true>(e); unit<true, 3, false, true, 10, true>(e);
    unit<true, 0, false, true, 8, true>(e); unit<true, 1, false, true, 6, true>(e);
    unit<true, 2, false, false, 6, true>(e); unit<true, 3, false, false, 4, true>(e);
    unit<true, 0, false, false, 2, true>(e); unit<true, 1, false, false, 0, false>(e);
    STAMP(12);
    lds_wait(Y_R2, 8, budget);
    dump_all(e, w, lane);
    lds_inc(Y_D4);
    STAMP(13);
    DUMPT();
    return;
  }

  const int j = w - 4;
  const rsrc_t wsr = make_rsrc(p.ws, WS_BYTES);
  const u32x4 raw = *(const u32x4*)(p.x + (koct * 8 + j) * D + lane * 8);
  const u32 tag0 = __builtin_amdgcn_raw_buffer_load_b32(wsr, OFF_E, 0, SC1);
  u32 i0 = 0, i1 = 0, i2 = 0;
  if (j == 1) { i0 = p.Lre[32 * g1 + l31]; i1 = p.Lim[32 * g1 + l31]; i2 = p.ldt[32 * g1 + l31]; }
  if (j == 0) i0 = p.b2[64 * st + lane];
  asm volatile("s_barrier" ::: "memory");
  __builtin_amdgcn_s_setprio(1);
  {
    float v[8];
#pragma unroll
    for (int i = 0; i < 4; i++) { v[2 * i] = bf2f(raw[i] & 0xffff); v[2 * i + 1] = bfhi(raw[i]); }
    const float piv = __builtin_bit_cast(float, __builtin_amdgcn_readfirstlane(__builtin_bit_cast(int, v[0])));
#pragma unroll
    for (int i = 0; i < 8; i++) v[i] -= piv;
    float sum = ((v[0] + v[1]) + (v[2] + v[3])) + ((v[4] + v[5]) + (v[6] + v[7]));
    float q = ((v[0] * v[0] + v[1] * v[1]) + (v[2] * v[2] + v[3] * v[3])) + ((v[4] * v[4] + v[5] * v[5]) + (v[6] * v[6] + v[7] * v[7]));
    wave_sum2(sum, q);
    const float ms = sum * (1.f / D), var = fmaxf(q * (1.f / D) - ms * ms, 0.f);
    const float r = __builtin_amdgcn_rsqf(var + 1e-5f);
    const float rr = r * __builtin_amdgcn_rsqf(var * r * r + 1e-5f);
    if (lane == 0) *LP(f32x2, L_ST0 + j * 8) = f32x2{ms + piv, r};
    u32x4 o;
#pragma unroll
    for (int i = 0; i < 4; i++) o[i] = pk2((v[2 * i] - ms) * rr, (v[2 * i + 1] - ms) * rr);
    *LP(u32x4, L_LNT + j * 1088 + lane * 16) = o;
    lds_inc(Y_LN);
  }
  STAMP(1);
  const u32 tag = uni(tag0) + 1u;
  {
    lds_wait<0>(Y_LN, 8, budget);
    const u32 a = L_LNT + ((lane & 15) >> 2) * 1088 + j * 128 + (lane >> 4) * 32 + (lane & 3) * 8;
    const bf16x8 t = tr_pair(a, a + 4 * 1088);
    const f32x2 sv = *LP(f32x2, L_ST0 + (lane & 7) * 8);
    __builtin_amdgcn_raw_buffer_store_b128(__builtin_bit_cast(u32x4, t), wsr, OFF_HN + j * 262144 + koct * 1024 + lane * 16, 0, SC1);
    if (j == 2 && lane < 8) { const u32x4 gr = {__float_as_uint(sv[0]), tag, __float_as_uint(sv[1]), tag};
                              __builtin_amdgcn_raw_buffer_store_b128(gr, wsr, OFF_STAT + (koct * 8 + lane) * 16, 0, SC1); }
    STAMP(2);
  }
  float a8r = 0, a8i = 0, qr = 0, qi = 0;
  if (j == 1) {
    float cr, ci;
    chan_const(i0, i1, i2, a8r, a8i, cr, ci);
    *LP(f32x4, L_CC + l31 * 16) = f32x4{a8r, a8i, cr, ci};
  }
  drain();
  if (lds_inc_rtn(Y_A) == 7 && lane < 8) __builtin_amdgcn_raw_buffer_store_b32(tag, wsr, OFF_FA + lane * 8192 + (koct >> 2) * 128 + (koct & 3) * 4, 0, SC1);
  STAMP(3);
  {
    lds_wait(Y_B, 4, budget);
    float s0 = 0, s1 = 0;
    bf16x2 one2 = {(__bf16)1.0f, (__bf16)1.0f};
#pragma unroll
    for (int k = 0; k < 8; k++) {
      const u32x4 t = *LP(u32x4, L_DUMP + k * 8192 + j * 1024 + lane * 16);
      const u32 t0 = t[0], t1 = t[1], t2 = t[2], t3 = t[3];
      s0 = __builtin_amdgcn_fdot2_f32_bf16(__builtin_bit_cast(bf16x2, t0), one2, s0, false);
      s1 = __builtin_amdgcn_fdot2_f32_bf16(__builtin_bit_cast(bf16x2, t1), one2, s1, false);
      s0 = __builtin_amdgcn_fdot2_f32_bf16(__builtin_bit_cast(bf16x2, t2), one2, s0, false);
      s1 = __builtin_amdgcn_fdot2_f32_bf16(__builtin_bit_cast(bf16x2, t3), one2, s1, false);
    }
    s0 += s1;
    s0 = dpp_add<0xB1, 0xf>(s0); s0 = dpp_add<0x4E, 0xf>(s0); s0 = dpp_add<0x141, 0xf>(s0);
    if ((lane & 7) == 0) *LP(float, L_BS + (8 * j + (lane >> 3)) * 4) = s0;
    lds_inc(Y_G1);
  }
  if (j == 1) {
    csq(a8r, a8i); csq(a8r, a8i); csq(a8r, a8i);
    qr = a8r; qi = a8i;
    csq(qr, qi); csq(qr, qi); csq(qr, qi);
    float tr = hh ? qr : 1.f, ti = hh ? qi : 0.f;
#pragma unroll
    for (int i = 0; i < 8; i++) { *LP(f32x2, L_PW + (8 * hh + i) * 256 + l31 * 8) = f32x2{tr, ti}; float t = a8r * tr - a8i * ti; ti = a8r * ti + a8i * tr; tr = t; }
  }
  if (j == 0) {
    lds_wait(Y_A, 8, budget);
    __builtin_amdgcn_s_sleep(D1);
    bool needA = act3, needS = true;
    for (;;) {
      u32x4 fa = {tag, tag, tag, tag}, a, c, d;
      if (needA) fa = __builtin_amdgcn_raw_buffer_load_b128(wsr, OFF_FA + xcd * 8192 + lane * 128, 0, SC1);
      if (needS) {
        a = __builtin_amdgcn_raw_buffer_load_b128(wsr, OFF_STAT + (128 * c1 + lane) * 16, 0, SC1);
        c = __builtin_amdgcn_raw_buffer_load_b128(wsr, OFF_STAT + (128 * c1 + 64 + lane) * 16, 0, SC1);
        d = __builtin_amdgcn_raw_buffer_load_b128(wsr, OFF_STAT + (64 * st + lane) * 16, 0, SC1);
      }
      const bool to = --budget < 0;
      if (needA && (__all(fa[0] == tag && fa[1] == tag && fa[2] == tag && fa[3] == tag) || to)) { lds_inc(Y_GATE1); needA = false; STAMP(14); }
      if (needS && (__all(a[1] == tag && a[3] == tag && c[1] == tag && c[3] == tag && d[1] == tag && d[3] == tag) || to)) {
        *LP(u32x2, L_ST1 + lane * 8) = u32x2{a[0], a[2]};
        *LP(u32x2, L_ST1 + 512 + lane * 8) = u32x2{c[0], c[2]};
        *LP(u32x4, L_ST2 + lane * 16) = u32x4{d[0], d[2], i0 << 16, 0u};
        lds_inc(Y_D1);
        needS = false;
      }
      if (!needA && !needS) break;
      __builtin_amdgcn_s_sleep(PSLEEP);
    }
    if (b == 0 && lane == 0) __builtin_amdgcn_raw_buffer_store_b32(tag, wsr, OFF_E, 0, SC1);
  }
  const int mi = j >> 2, ni = (j >> 1) & 1, rh = j & 1;
  const int rl = 32 * mi + 16 * hh + 8 * rh;
  const int f0 = 64 * ft + rl, s0 = 64 * st + rl, dd = 64 * dt + 32 * ni + l31;
  u32x4 b1v = {0, 0, 0, 0};
  if (act3 && f0 < F) b1v = *(const u32x4*)(p.b1 + f0);
  const rsrc_t xr = make_rsrc(p.x, S * D * 2), outr = make_rsrc(p.out, S * D * 4);
  float xv[8];
#pragma unroll
  for (int i = 0; i < 8; i++) xv[i] = bf2f(__builtin_amdgcn_raw_buffer_load_b16(xr, (s0 * D + dd) * 2, i * 1024, 0));
  const float dsk = bf2f(p.Dsk[dd]);
  {
    lds_wait(Y_D1, 5, budget);
    STAMP(4);
    const int en = j >> 1, seg = 4 * en + 2 * hh + rh;
    const u32 da = L_DUMP + en * 8192 + rh * 2048 + lane * 16;
    const f32x4 pr0 = *LP(f32x4, da), pr1 = *LP(f32x4, da + 1024), pi0 = *LP(f32x4, da + 4096), pi1 = *LP(f32x4, da + 5120);
    const f32x4 cc = *LP(f32x4, L_CC + l31 * 16);
    const float bsr = *LP(float, L_BS + l31 * 4), bsi = *LP(float, L_BS + 128 + l31 * 4);
    const float ar = cc[0], ai = cc[1], cr = cc[2], ci = cc[3];
    float vr[8], vi[8], er = 0, ei = 0;
#pragma unroll
    for (int i2_ = 0; i2_ < 4; i2_++) {
      const f32x4 sv = *LP(f32x4, L_ST1 + seg * 64 + i2_ * 16);
#pragma unroll
      for (int u = 0; u < 2; u++) {
        const int i = 2 * i2_ + u;
        const float a_ = i < 4 ? pr0[i & 3] : pr1[i & 3], b_ = i < 4 ? pi0[i & 3] : pi1[i & 3];
        const float pr = (a_ - sv[2 * u] * bsr) * sv[2 * u + 1], pq = (b_ - sv[2 * u] * bsi) * sv[2 * u + 1];
        vr[i] = cr * pr - ci * pq; vi[i] = cr * pq + ci * pr;
        const float t = ar * er - ai * ei + vr[i]; ei = ar * ei + ai * er + vi[i]; er = t;
      }
    }
    *LP(f32x2, L_SEG + seg * 256 + l31 * 8) = f32x2{er, ei};
    lds_inc(Y_E);
    STAMP(5);
    if (j == 1) {
      float br = qr, bi = qi;
      csq(br, bi);
      float dr = br, di = bi;
      csq(dr, di);
      lds_wait<0>(Y_E, 8, budget);
      f32x2 ev[8], pf[8];
#pragma unroll
      for (int i = 0; i < 8; i++) ev[i] = *LP(f32x2, L_SEG + (8 * hh + i) * 256 + l31 * 8);
      float gr_ = 0, gi_ = 0;
#pragma unroll
      for (int i = 0; i < 8; i++) { pf[i] = f32x2{gr_, gi_}; float t = a8r * gr_ - a8i * gi_ + ev[i][0]; gi_ = a8r * gi_ + a8i * gr_ + ev[i][1]; gr_ = t; }
      auto sw0 = __builtin_amdgcn_permlane32_swap(__float_as_uint(gr_), __float_as_uint(gr_), false, false);
      auto sw1 = __builtin_amdgcn_permlane32_swap(__float_as_uint(gi_), __float_as_uint(gi_), false, false);
      float lr_ = hh ? __uint_as_float(sw0[0]) : 0.f, li_ = hh ? __uint_as_float(sw1[0]) : 0.f;
      if (hh) { const u32x4 g4 = {__float_as_uint(gr_ + qr * lr_ - qi * li_), tag, __float_as_uint(gi_ + qr * li_ + qi * lr_), tag};
                __builtin_amdgcn_raw_buffer_store_b128(g4, wsr, OFF_AGG + (c1 * 512 + 32 * g1 + l31) * 16, 0, SC1); }
      STAMP(14);
#pragma unroll
      for (int i = 0; i < 8; i++) {
        *LP(f32x2, L_CIN + (8 * hh + i) * 256 + l31 * 8) = f32x2{pf[i][0] + lr_, pf[i][1] + li_};
        float t = a8r * lr_ - a8i * li_; li_ = a8r * li_ + a8i * lr_; lr_ = t;
      }
      drain();
      __builtin_amdgcn_s_sleep(D2);
      float hr, hi;
      for (;;) {
        u32x4 a[8]; bool ok = true;
#pragma unroll
        for (int i = 0; i < 8; i++)
          a[i] = __builtin_amdgcn_raw_buffer_load_b128(wsr, 2 * i + hh < c1 ? OFF_AGG + ((2 * i + hh) * 512 + 32 * g1 + l31) * 16 : WS_BYTES, 0, SC1);
        hr = 0; hi = 0;
#pragma unroll
        for (int i = 0; i < 8; i++) {
          const bool use = 2 * i + hh < c1;
          ok = ok && (!use || (a[i][1] == tag && a[i][3] == tag));
          float t = dr * hr - di * hi + __uint_as_float(a[i][0]), u = dr * hi + di * hr + __uint_as_float(a[i][2]);
          hr = use ? t : hr; hi = use ? u : hi;
        }
        if (__all(ok) || --budget < 0) break;
        __builtin_amdgcn_s_sleep(PSLEEP);
      }
      if (((c1 - 1) & 1) != hh) { float t = br * hr - bi * hi; hi = br * hi + bi * hr; hr = t; }
      auto s0_ = __builtin_amdgcn_permlane32_swap(__float_as_uint(hr), __float_as_uint(hr), false, false);
      auto s1_ = __builtin_amdgcn_permlane32_swap(__float_as_uint(hi), __float_as_uint(hi), false, false);
      *LP(f32x2, L_H + l31 * 8) = f32x2{__uint_as_float(s0_[0]) + __uint_as_float(s0_[1]), __uint_as_float(s1_[0]) + __uint_as_float(s1_[1])};
      lds_inc(Y_CIN);
    }
    lds_wait<2>(Y_CIN, 1, budget);
    STAMP(6);
    const f32x2 cv = *LP(f32x2, L_CIN + seg * 256 + l31 * 8), pw = *LP(f32x2, L_PW + seg * 256 + l31 * 8), hv = *LP(f32x2, L_H + l31 * 8);
    float hr = cv[0] + pw[0] * hv[0] - pw[1] * hv[1], hi = cv[1] + pw[0] * hv[1] + pw[1] * hv[0];
#pragma unroll
    for (int i = 0; i < 8; i++) { float t = ar * hr - ai * hi + vr[i]; hi = ar * hi + ai * hr + vi[i]; hr = t; vr[i] = hr; vi[i] = -hi; }
    u32x4 o0, o1;
#pragma unroll
    for (int i = 0; i < 4; i++) { o0[i] = pk2(vr[2 * i], vr[2 * i + 1]); o1[i] = pk2(vi[2 * i], vi[2 * i + 1]); }
    const u32 sc = L_DUMP + 32768 + j * 2048;
    *LP(u32x4, sc + l31 * 32 + hh * 16) = o0;
    *LP(u32x4, sc + 1024 + l31 * 32 + hh * 16) = o1;
    cfence();
    const u32 ta = sc + (8 * (lane >> 4) + ((lane & 15) >> 2)) * 32 + (lane & 3) * 8;
    const bf16x8 t0 = tr_pair(ta, ta + 128), t1 = tr_pair(ta + 1024, ta + 1024 + 128);
    const int tk = 32 * (en & 1) + 16 * ((lane >> 3) & 1) + 8 * rh + (lane & 7);
    const u32 off = OFF_HX + (2 * c1 + (en >> 1)) * 131072 + (4 * g1 + (lane >> 4)) * 1024 + tk * 16;
    __builtin_amdgcn_raw_buffer_store_b128(__builtin_bit_cast(u32x4, t0), wsr, off, 0, SC1);
    __builtin_amdgcn_raw_buffer_store_b128(__builtin_bit_cast(u32x4, t1), wsr, off, 65536, SC1);
    lds_inc(Y_HS);
    drain();
    const u32x2 f2 = {tag, tag};
    if (lds_inc_rtn(Y_H) == 7 && lane == 0) __builtin_amdgcn_raw_buffer_store_b64(f2, wsr, OFF_FH + (c1 * 16 + g1) * 128, 0, SC1);
    STAMP(7);
  }
  if (j == 0) {
    __builtin_amdgcn_s_sleep(DH);
    for (;;) {
      u32x2 a = __builtin_amdgcn_raw_buffer_load_b64(wsr, OFF_FH + (st >> 1) * 2048 + (lane & 15) * 128, 0, SC1);
      if (__all(a[0] == tag && a[1] == tag) || --budget < 0) break;
      __builtin_amdgcn_s_sleep(2 * PSLEEP);
    }
    lds_inc(Y_GATE2);
    STAMP(15);
  }
  if (act3) {
    lds_wait(Y_D3, 4, budget);
    STAMP(8);
    float o[8];
    gather(o, j, lane);
    lds_inc(Y_R3);
#pragma unroll
    for (int i = 0; i < 8; i++) {
      const float a = o[i] + ((i & 1) ? bfhi(b1v[i >> 1]) : bf2f(b1v[i >> 1] & 0xffff));
      const float z = -2.302208198f * (a + 0.044715f * a * a * a);
      o[i] = a * __builtin_amdgcn_rcpf(1.f + __builtin_amdgcn_exp2f(z));
    }
    u32x4 uo;
#pragma unroll
    for (int i = 0; i < 4; i++) uo[i] = f0 < F ? pk2(o[2 * i], o[2 * i + 1]) : 0u;
    __builtin_amdgcn_raw_buffer_store_b128(uo, wsr, OFF_U + dt3 * 180224 + (f0 >> 3) * 1024 + (32 * ni + l31) * 16, 0, SC1);
    drain();
    const u32x2 f2 = {tag, tag};
    if (lds_inc_rtn(Y_U) == 7 && lane < 4) __builtin_amdgcn_raw_buffer_store_b64(f2, wsr, OFF_FU + lane * 32768 + dt3 * 4096 + ft * 128, 0, SC1);
    STAMP(9);
  }
  if (j == 0) {
    if (act3) __builtin_amdgcn_s_sleep(D3); else __builtin_amdgcn_s_sleep(100);
    for (;;) {
      u32x2 a = __builtin_amdgcn_raw_buffer_load_b64(wsr, OFF_FU + (xcd >> 1) * 32768 + dt * 4096 + min(lane, 21) * 128, 0, SC1);
      if (__all(a[0] == tag && a[1] == tag) || --budget < 0) break;
      __builtin_amdgcn_s_sleep(PSLEEP);
    }
    lds_inc(Y_GATE3);
    STAMP(10);
  }
  float sp[8], b2v[8];
  lds_wait<2>(Y_D2, 4, budget);
  gather(sp, j, lane);
  lds_inc(Y_R2);
#pragma unroll
  for (int i = 0; i < 8; i++) {
    const f32x4 sv = *LP(f32x4, L_ST2 + (rl + i) * 16);
    sp[i] += (xv[i] - sv[0]) * sv[1] * dsk; b2v[i] = sv[2];
    asm volatile("" : "+v"(sp[i]), "+v"(b2v[i]), "+v"(xv[i]));
  }
  STAMP(11);
  lds_wait(Y_D4, 4, budget);
  STAMP(12);
  {
    float o[8];
    gather(o, j, lane);
#pragma unroll
    for (int i = 0; i < 8; i++) {
      const float gv = o[i] + b2v[i];
      const float sg = gv * __builtin_amdgcn_rcpf(1.f + __builtin_amdgcn_exp2f(-1.442695041f * gv));
      __builtin_amdgcn_raw_buffer_store_b32(__float_as_uint(xv[i] + sp[i] * sg), outr, (s0 * D + dd) * 4, i * 2048, 2);
    }
  }
  STAMP(13);
  DUMPT();
}

#if defined(__HIP_DEVICE_COMPILE__) && __HIP_DEVICE_COMPILE__
#pragma clang attribute pop
#endif

extern "C" void kernel_launch(void* const* d_in, const int* in_sizes, int n_in, void* d_out, int out_size, void* d_ws,
                              size_t ws_size, hipStream_t stream) {
  Params p;
  p.x = (const u16*)d_in[0]; p.Lre = (const u16*)d_in[1]; p.Lim = (const u16*)d_in[2];
  p.Bre = (const u16*)d_in[3]; p.Bim = (const u16*)d_in[4]; p.Cre = (const u16*)d_in[5]; p.Cim = (const u16*)d_in[6];
  p.ldt = (const u16*)d_in[7]; p.Dsk = (const u16*)d_in[8];
  p.w1 = (const u16*)d_in[10]; p.b1 = (const u16*)d_in[11]; p.w2 = (const u16*)d_in[12]; p.b2 = (const u16*)d_in[13];
  p.out = (float*)d_out; p.ws = (char*)d_ws;
  (void)hipFuncSetAttribute((const void*)s5_mixer_kernel, hipFuncAttributeMaxDynamicSharedMemorySize, LDS_BYTES);
  s5_mixer_kernel<<<256, NWAVE * 64, LDS_BYTES, stream>>>(p);
}
```

```cpp
#if defined(__HIP_DEVICE_COMPILE__) && __HIP_DEVICE_COMPILE__
#pragma clang attribute push(__attribute__((target("no-packed-fp32-ops"))), apply_to = function)
#endif
#include <hip/hip_runtime.h>
#include <stdint.h>

typedef unsigned short u16;
typedef unsigned int u32;
typedef unsigned long long u64;
typedef __attribute__((ext_vector_type(2))) unsigned u32x2;
typedef __attribute__((ext_vector_type(4))) unsigned u32x4;
typedef __attribute__((ext_vector_type(2))) float f32x2;
typedef __attribute__((ext_vector_type(4))) float f32x4;
typedef __attribute__((ext_vector_type(16))) float f32x16;
typedef __attribute__((ext_vector_type(2))) __bf16 bf16x2;
typedef __attribute__((ext_vector_type(4))) __bf16 bf16x4;
typedef __attribute__((ext_vector_type(8))) __bf16 bf16x8;
#define LAS __attribute__((address_space(3)))
#define LP(T, a) ((LAS T*)(a))
#define VLP(T, a) ((volatile LAS T*)(a))
#define DEV __device__ __forceinline__
#define SB() __builtin_amdgcn_sched_barrier(0)

#define DIAG 0
#define D1 28
#define D2 14
#define DH 16
#define D3 12
#define PSLEEP 8
#define ABL 0
#define PF0 0
#define PF 29
#define OUTAUX 2
#define ROTB (idx >> 3)
#define ROT1 0

constexpr int S = 2048, D = 512, P = 512, F = 1376;
constexpr int NWAVE = 12;
constexpr u32 OFF_E = 0, OFF_STAT = 32768, OFF_AGG = 65536, OFF_FA = 196608, OFF_FH = 262144, OFF_FU = 294912;
constexpr u32 OFF_HN = 524288;
constexpr u32 OFF_HX = OFF_HN + (2u << 20);
constexpr u32 OFF_U = OFF_HX + (4u << 20);
constexpr u32 WS_BYTES = OFF_U + 8u * 176u * 1024u;
constexpr u32 OFF_DIAG = 16u << 20;
constexpr int R = 5;
constexpr u32 L_DUMP = 4 * R * 4096, L_LNT = L_DUMP + 65536, L_SEG = L_LNT, L_CIN = L_LNT + 4096, L_PW = L_LNT + 8704, L_ST1 = L_PW + 4096,
              L_ST2 = L_ST1 + 1024, L_CC = L_ST2 + 1024, L_H = L_CC + 512, L_BS = L_H + 256, L_ST0 = L_BS + 256, L_SYNC = L_ST0 + 64,
              LDS_BYTES = L_SYNC + 256;
enum { Y_LN = 0, Y_A, Y_B, Y_BS, Y_G1, Y_D1, Y_ST, Y_E, Y_CIN, Y_HS, Y_H, Y_D3, Y_R3, Y_U, Y_D2, Y_R2, Y_D4, Y_GATE1, Y_GATE2, Y_GATE3 };
#define YA(i) (L_SYNC + 4 * (i))
constexpr int SC1 = 16;

struct Params {
  const u16 *x, *Lre, *Lim, *Bre, *Bim, *Cre, *Cim, *ldt, *Dsk, *w1, *b1, *w2, *b2;
  float* out;
  char* ws;
};

DEV float bf2f(u32 v) { return __uint_as_float(v << 16); }
DEV float bfhi(u32 v) { return __uint_as_float(v & 0xffff0000u); }
DEV u32 pk2(float a, float b) { f32x2 t = {a, b}; bf16x2 r = __builtin_convertvector(t, bf16x2); return __builtin_bit_cast(u32, r); }
DEV int uni(int v) { return __builtin_amdgcn_readfirstlane(v); }
DEV void cfence() { asm volatile("" ::: "memory"); }
template <int CTRL, int RM> DEV float dpp_add(float x) {
  return x + __builtin_bit_cast(float, __builtin_amdgcn_update_dpp(0, __builtin_bit_cast(int, x), CTRL, RM, 0xf, false));
}
DEV void wave_sum2(float& x, float& y) {
  x = dpp_add<0xB1, 0xf>(x); y = dpp_add<0xB1, 0xf>(y); x = dpp_add<0x4E, 0xf>(x); y = dpp_add<0x4E, 0xf>(y);
  x = dpp_add<0x141, 0xf>(x); y = dpp_add<0x141, 0xf>(y); x = dpp_add<0x140, 0xf>(x); y = dpp_add<0x140, 0xf>(y);
  x = dpp_add<0x142, 0xa>(x); y = dpp_add<0x142, 0xa>(y); x = dpp_add<0x143, 0xc>(x); y = dpp_add<0x143, 0xc>(y);
  x = __builtin_bit_cast(float, __builtin_amdgcn_readlane(__builtin_bit_cast(int, x), 63));
  y = __builtin_bit_cast(float, __builtin_amdgcn_readlane(__builtin_bit_cast(int, y), 63));
}
DEV u32x4 make_srd(const void* p, u32 bytes) {
  u64 a = (u64)p; u32x4 r = {(u32)a, (u32)(a >> 32) & 0xffffu, bytes, 0x00020000u}; return r;
}
typedef __amdgpu_buffer_rsrc_t rsrc_t;
DEV rsrc_t make_rsrc(const void* p, u32 bytes) { return __builtin_amdgcn_make_buffer_rsrc((void*)p, 0, bytes, 0x00020000); }

template <int SL = 1> DEV void lds_wait(int y, int target, int& budget) {
  u32 t;
  budget = uni(budget);
  asm volatile(
      "1:\n\t"
      "ds_read_b32 %[t], %[a]\n\t"
      "s_sub_i32 %[bud], %[bud], 1\n\t"
      "s_cmp_lt_i32 %[bud], 0\n\t"
      "s_waitcnt lgkmcnt(0)\n\t"
      "v_cmp_le_i32 vcc, %[tg], %[t]\n\t"
      "s_cbranch_vccnz 2f\n\t"
      "s_sleep %[sl]\n\t"
      "s_cbranch_scc0 1b\n"
      "2:"
      : [t] "=&v"(t), [bud] "+s"(budget) : [a] "v"(YA(y)), [tg] "s"(target), [sl] "n"(SL) : "vcc", "scc", "memory");
}
DEV void lds_inc(int y) {
  asm volatile("s_mov_b64 exec, 1\n\tds_add_u32 %0, %1\n\ts_mov_b64 exec, -1" :: "v"(YA(y)), "v"(1) : "memory");
}
DEV int lds_inc_rtn(int y) {
  int old;
  asm volatile("s_mov_b64 exec, 1\n\tds_add_rtn_u32 %0, %1, %2\n\ts_mov_b64 exec, -1\n\ts_waitcnt lgkmcnt(0)" : "=&v"(old) : "v"(YA(y)), "v"(1) : "memory");
  return uni(old);
}
DEV void drain() { asm volatile("s_waitcnt vmcnt(0)" ::: "memory"); }
DEV bf16x8 tr_pair(u32 a0, u32 a1) {
  bf16x4 lo = __builtin_amdgcn_ds_read_tr16_b64_v4bf16(LP(bf16x4, a0));
  bf16x4 hi = __builtin_amdgcn_ds_read_tr16_b64_v4bf16(LP(bf16x4, a1));
  return __builtin_shufflevector(lo, hi, 0, 1, 2, 3, 4, 5, 6, 7);
}
#define STAMP(i) do {} while (0)
#define DUMPT() do {} while (0)

template <int O> DEV void dma_m(u32x4 srd, u32 v, u32 so, u32 m0v) {
  asm volatile("s_mov_b32 m0, %3\n\ts_nop 0\n\tbuffer_load_dwordx4 %0, %1, %2 offen offset:%4 lds" :: "v"(v), "s"(srd), "s"(so), "s"(m0v), "n"(O) : "memory");
}
template <int O> DEV void dma_n(u32x4 srd, u32 v, u32 so) {
  asm volatile("buffer_load_dwordx4 %0, %1, %2 offen offset:%3 lds" :: "v"(v), "s"(srd), "s"(so), "n"(O) : "memory");
}
template <int O> DEV void xload(u32x4& d, u32x4 srd, u32 v, u32 so) {
  asm volatile("buffer_load_dwordx4 %0, %1, %2, %3 offen offset:%4 sc1" : "=v"(d) : "v"(v), "s"(srd), "s"(so), "n"(O) : "memory");
}
template <int O> DEV void xload_if(u32x4& d, u32x4 srd, u32 v, u32 so, int on) {
  asm volatile("s_cmp_eq_u32 %5, 0\n\ts_cbranch_scc1 1f\n\tbuffer_load_dwordx4 %0, %1, %2, %3 offen offset:%4 sc1\n1:"
               : "+v"(d) : "v"(v), "s"(srd), "s"(so), "n"(O), "s"(on) : "scc", "memory");
}
template <int N> DEV void xwait(u32x4& a, u32x4& b) { asm volatile("s_waitcnt vmcnt(%2)" : "+v"(a), "+v"(b) : "n"(N) : "memory"); }
#define MFMA32(c, a, b) c = __builtin_amdgcn_mfma_f32_32x32x16_bf16(a, b, c, 0, 0, 0)
DEV bf16x8 asbf(u32x4 v) { return __builtin_bit_cast(bf16x8, v); }

struct Eng {
  u32x4 wsrd, xsrd;
  u32 wv[4], wso, wstep;
  u32 wdst, rsl, rlo, rhi;
  u32 xv, xso, xs1, xs2;
  u32 aW, aC;
  u32x4 x[4][2];
  bf16x8 a[2][2];
  f32x16 c[2][2];
};
DEV u32 nxt(const Eng& e, u32 s) { s += 4096; return s == e.rhi ? e.rlo : s; }
DEV void w_slot(Eng& e) {
  dma_m<0>(e.wsrd, e.wv[0], e.wso, e.wdst); dma_n<1024>(e.wsrd, e.wv[1], e.wso); dma_n<2048>(e.wsrd, e.wv[2], e.wso); dma_n<3072>(e.wsrd, e.wv[3], e.wso);
  e.wso += e.wstep; e.wdst = nxt(e, e.wdst);
}
DEV void w_set(Eng& e, u32x4 srd, u32 v0, u32 v1, u32 pitch8, u32 so, u32 step) {
  e.wsrd = srd; e.wv[0] = v0; e.wv[1] = v1 + pitch8 - 1024; e.wv[2] = v0 + 2 * pitch8 - 2048; e.wv[3] = v1 + 3 * pitch8 - 3072; e.wso = so; e.wstep = step;
}
template <bool TR, int U, int H> DEV void frag_half(Eng& e) {
  if (TR) {
    const u32 b = e.rsl + (e.aW ^ (H * 64));
    e.a[U & 1][H] = tr_pair(b + (U & 1) * 2048, b + (U & 1) * 2048 + 512);
  } else {
    e.a[U & 1][H] = *LP(bf16x8, (H ? nxt(e, e.rsl) : e.rsl) + (e.aC ^ (U << 5)));
  }
}
template <bool TR, int U> DEV void frag_read(Eng& e) { frag_half<TR, U, 0>(e); frag_half<TR, U, 1>(e); }
template <bool TR, int U, bool DOW, int DOX, int VM, bool NEXT>
DEV void unit(Eng& e, int xon = 1) {
  xwait<VM>(e.x[U][0], e.x[U][1]);
  if (TR) { if (U & 1) e.rsl = nxt(e, e.rsl); }
  else if (U == 3) e.rsl = nxt(e, nxt(e, e.rsl));
  constexpr bool LATE = !TR && U == 2;
  const bf16x8 f0 = e.a[U & 1][0], f1 = e.a[U & 1][1], x0 = asbf(e.x[U][0]), x1 = asbf(e.x[U][1]);
  SB();
  if (TR) MFMA32(e.c[0][0], f0, x0); else MFMA32(e.c[0][0], x0, f0);
  SB();
  if (NEXT) frag_half<TR, (U + 1) & 3, 0>(e);
  if (DOW && !LATE) { if (U & 1) dma_n<2048>(e.wsrd, e.wv[2], e.wso); else dma_m<0>(e.wsrd, e.wv[0], e.wso, e.wdst); }
  SB();
  if (TR) MFMA32(e.c[1][0], f1, x0); else MFMA32(e.c[0][1], x0, f1);
  SB();
  if (NEXT) frag_half<TR, (U + 1) & 3, 1>(e);
  if (DOX == 1) xload<(U & 1) * 2048>(e.x[U][0], e.xsrd, e.xv, e.xso);
  if (DOX == 2) xload_if<(U & 1) * 2048>(e.x[U][0], e.xsrd, e.xv, e.xso, xon);
  SB();
  if (TR) MFMA32(e.c[0][1], f0, x1); else MFMA32(e.c[1][0], x1, f0);
  SB();
  if (DOW && !LATE) { if (U & 1) dma_n<3072>(e.wsrd, e.wv[3], e.wso); else dma_n<1024>(e.wsrd, e.wv[1], e.wso); }
  SB();
  MFMA32(e.c[1][1], TR ? f1 : x1, TR ? x1 : f1);
  SB();
  if (DOX == 1) xload<(U & 1) * 2048 + 512>(e.x[U][1], e.xsrd, e.xv, e.xso);
  if (DOX == 2) xload_if<(U & 1) * 2048 + 512>(e.x[U][1], e.xsrd, e.xv, e.xso, xon);
  if (DOW && LATE) { asm volatile("s_waitcnt lgkmcnt(0)" ::: "memory"); dma_m<0>(e.wsrd, e.wv[0], e.wso, e.wdst); dma_n<1024>(e.wsrd, e.wv[1], e.wso); }
  if (U & 1) {
    if (DOX) e.xso += U == 1 ? e.xs1 : e.xs2;
    if (DOW) { e.wso += e.wstep; e.wdst = nxt(e, e.wdst); }
  }
  SB();
}
template <bool TR, bool NEXT> DEV void group(Eng& e) {
  unit<TR, 0, true, 1, 12, true>(e); unit<TR, 1, true, 1, 12, true>(e); unit<TR, 2, true, 1, 12, true>(e); unit<TR, 3, true, 1, 12, NEXT>(e);
}
template <bool TR> DEV void group_first(Eng& e) {
  xwait<6>(e.x[0][0], e.x[0][1]);
  frag_read<TR, 0>(e);
  unit<TR, 0, true, 1, 6, true>(e); unit<TR, 1, true, 1, 8, true>(e); unit<TR, 2, true, 1, 10, true>(e); unit<TR, 3, true, 1, 12, true>(e);
}
template <bool TR> DEV void group_maybe(Eng& e, int on) {
  unit<TR, 0, true, 2, 12, true>(e, on); unit<TR, 1, true, 2, 10, true>(e, on); unit<TR, 2, true, 2, 8, true>(e, on); unit<TR, 3, true, 2, 6, false>(e, on);
}
DEV void x_prologue_unless(Eng& e, u32 so0, u32 so1, int on) {
  asm volatile("s_cmp_lg_u32 %12, 0\n\ts_cbranch_scc1 1f\n\t"
               "buffer_load_dwordx4 %0, %8, %9, %10 offen sc1\n\tbuffer_load_dwordx4 %1, %8, %9, %10 offen offset:512 sc1\n\t"
               "buffer_load_dwordx4 %2, %8, %9, %10 offen offset:2048 sc1\n\tbuffer_load_dwordx4 %3, %8, %9, %10 offen offset:2560 sc1\n\t"
               "buffer_load_dwordx4 %4, %8, %9, %11 offen sc1\n\tbuffer_load_dwordx4 %5, %8, %9, %11 offen offset:512 sc1\n\t"
               "buffer_load_dwordx4 %6, %8, %9, %11 offen offset:2048 sc1\n\tbuffer_load_dwordx4 %7, %8, %9, %11 offen offset:2560 sc1\n1:"
               : "+v"(e.x[0][0]), "+v"(e.x[0][1]), "+v"(e.x[1][0]), "+v"(e.x[1][1]), "+v"(e.x[2][0]), "+v"(e.x[2][1]), "+v"(e.x[3][0]), "+v"(e.x[3][1])
               : "v"(e.xv), "s"(e.xsrd), "s"(so0), "s"(so1), "s"(on) : "scc", "memory");
}
DEV void x_prologue(Eng& e) {
  xload<0>(e.x[0][0], e.xsrd, e.xv, e.xso); xload<512>(e.x[0][1], e.xsrd, e.xv, e.xso);
  xload<2048>(e.x[1][0], e.xsrd, e.xv, e.xso); xload<2560>(e.x[1][1], e.xsrd, e.xv, e.xso);
  e.xso += e.xs1;
  xload<0>(e.x[2][0], e.xsrd, e.xv, e.xso); xload<512>(e.x[2][1], e.xsrd, e.xv, e.xso);
  xload<2048>(e.x[3][0], e.xsrd, e.xv, e.xso); xload<2560>(e.x[3][1], e.xsrd, e.xv, e.xso);
  e.xso += e.xs2;
}
DEV void dump16(f32x16& c, u32 a) {
#pragma unroll
  for (int g = 0; g < 4; g++) *LP(f32x4, a + g * 1024) = f32x4{c[4 * g], c[4 * g + 1], c[4 * g + 2], c[4 * g + 3]};
  c = f32x16{};
}
DEV void dump_all(Eng& e, int w, int lane) {
  const u32 a = L_DUMP + w * 16384 + lane * 16;
  dump16(e.c[0][0], a); dump16(e.c[0][1], a + 4096); dump16(e.c[1][0], a + 8192); dump16(e.c[1][1], a + 12288);
}
DEV void gather(float (&o)[8], int j, int lane) {
  const u32 a = L_DUMP + (j >> 1) * 4096 + (j & 1) * 2048 + lane * 16;
  f32x4 t[4][2];
#pragma unroll
  for (int e = 0; e < 4; e++) { t[e][0] = *LP(f32x4, a + e * 16384); t[e][1] = *LP(f32x4, a + e * 16384 + 1024); }
#pragma unroll
  for (int i = 0; i < 8; i++) o[i] = (t[0][i >> 2][i & 3] + t[1][i >> 2][i & 3]) + (t[2][i >> 2][i & 3] + t[3][i >> 2][i & 3]);
}
DEV void chan_const(u32 lre, u32 lim, u32 ldt, float& ar, float& ai, float& cr, float& ci) {
  const float lr = bf2f(lre), li = bf2f(lim), dt = __builtin_amdgcn_exp2f(1.4426950409f * bf2f(ldt));
  const float xr = dt * lr, rev = dt * li * 0.15915494309f;
  const float e = __builtin_amdgcn_exp2f(1.4426950409f * xr), em1 = expm1f(xr);
  const float fr = rev - rintf(rev);
  const float sn = __builtin_amdgcn_sinf(fr), cs = __builtin_amdgcn_cosf(fr), sh = __builtin_amdgcn_sinf(0.5f * fr);
  ar = e * cs; ai = e * sn;
  const float nr = em1 * cs - 2.f * sh * sh, ni = ai, den = 2.f / (lr * lr + li * li);
  cr = (nr * lr + ni * li) * den; ci = (ni * lr - nr * li) * den;
}
DEV void csq(float& r, float& i) { float t = r * r - i * i; i = 2.f * r * i; r = t; }

__global__ __launch_bounds__(NWAVE * 64) void s5_mixer_kernel(Params p) {
  const int tid = __builtin_amdgcn_workitem_id_x(), lane = tid & 63, w = uni(tid >> 6), b = __builtin_amdgcn_workgroup_id_x();
  const int xcd = b & 7, idx = b >> 3;
  const int koct = xcd * 32 + idx;
  const int c1 = 4 * (xcd >> 1) + (idx >> 3), g1 = 8 * (xcd & 1) + (idx & 7);
  const bool act3 = idx < 22;
  const int t3 = (xcd >> 1) * 22 + idx, ft = t3 >> 2, dt3 = 4 * (xcd & 1) + (t3 & 3);
  const int st = 8 * (xcd >> 1) + (idx >> 2), dt = 4 * (xcd & 1) + (idx & 3);
  const int hh = lane >> 5, l31 = lane & 31;
  int budget = 1 << 20;
  STAMP(0);

  if (w < 4) {
    Eng e;
    const u32 rw = lane >> 3, pos = lane & 7;
    const u32 swzR = (pos ^ (lane >> 4)) << 4, swzW = (pos ^ (((lane >> 4) & 1) << 2)) << 4;
    e.rlo = w * R * 4096; e.rhi = e.rlo + R * 4096; e.wdst = e.rlo; e.rsl = e.rlo;
    if (w == 0) *VLP(int, L_SYNC + lane * 4) = 0;
    { const u32 v0 = (16 * hh + (rw & 3)) * 1024 + swzR; w_set(e, make_srd(p.x, S * D * 2), v0, v0 ^ 64, 4096, (128 * c1 + 32 * w) * 1024, 128); }
    const u32x4 sb = make_srd(w < 2 ? p.Bre : p.Bim, P * D * 2);
    const u32 bv0 = rw * 1024 + swzR, bv1 = (bv0 ^ 64) + 7168, bbase = (32 * g1 + 16 * (w & 1)) * 1024, rotb = ROTB;
    auto b_panel = [&](int k) {
      const u32 kp = (k & 4) | ((k + rotb) & 3);
      dma_m<0>(sb, bv0, bbase + kp * 128, L_DUMP + kp * 8192 + w * 2048);
      dma_n<1024>(sb, bv1, bbase + kp * 128);
    };
    asm volatile("s_waitcnt lgkmcnt(0)\n\ts_barrier" ::: "memory");
    b_panel(0); b_panel(1); b_panel(2); b_panel(3);
    w_slot(e); w_slot(e); w_slot(e);
    STAMP(1);
    e.xsrd = make_srd(p.ws, WS_BYTES);
    e.aW = (8 * hh + ((lane & 15) >> 2)) * 128 + (((2 * (lane & 1) + ((lane >> 4) & 1)) ^ (((lane >> 3) & 1) << 2)) << 4) + ((lane >> 1) & 1) * 8;
    e.aC = l31 * 128 + ((hh ^ ((l31 >> 1) & 7)) << 4);
    auto set_w1 = [&]() { const u32 v = rw * 2752 + (ft == 21 ? (swzW & 48) : swzW); w_set(e, make_srd(p.w1, S * F * 2), v, v, 8 * 2752, w * 32 * 2752 + ft * 128, 128 * 2752); };
    auto set_w2 = [&]() { const u32 v = rw * 4096 + swzW; w_set(e, make_srd(p.w2, F * S * 2), v, v, 32768, w * 32 * 4096 + st * 128, 128 * 4096); };
    const u32 cbase = dt * 65536 + (w & 1) * 512;
    auto set_c = [&]() { const u32 v = rw * 1024 + swzR; w_set(e, make_srd(w < 2 ? p.Cre : p.Cim, D * P * 2), v, v ^ 64, 8192, cbase, 32768); };
    auto c_wrap = [&]() { if (e.wso >= cbase + 65536) e.wso += 128 - 65536; };
    asm volatile("s_waitcnt vmcnt(12)" ::: "memory");
    lds_inc(Y_B);
    e.c[0][0] = f32x16{}; e.c[0][1] = f32x16{}; e.c[1][0] = f32x16{}; e.c[1][1] = f32x16{};
    lds_wait<0>(Y_B, 4, budget);
    STAMP(2);
    {
      bf16x8 fa[4], fb[4][2];
      u32 ra[4], rb[4];
#pragma unroll
      for (int s = 0; s < 4; s++) { ra[s] = e.rlo + (e.aC ^ (s << 5)); rb[s] = L_DUMP + (e.aC ^ (s << 5)); asm volatile("" : "+v"(rb[s])); }
#define G1_A(u) fa[(u) & 3] = *LP(bf16x8, ra[(u) & 3] + (((u) >> 2) % R) * 4096)
#define G1_B(u, h) fb[(u) & 3][h] = *LP(bf16x8, rb[(u) & 3] + ((u) >> 2) * 8192 + (h) * 4096)
      asm volatile("s_waitcnt vmcnt(8)" ::: "memory");
#pragma unroll
      for (int u = 0; u < 3; u++) { G1_A(u); G1_B(u, 0); G1_B(u, 1); }
#pragma unroll
      for (int u = 0; u < 32; u++) {
        if (u == 1) asm volatile("s_waitcnt vmcnt(6)" ::: "memory");
        if (u == 5 || u == 13) asm volatile("s_waitcnt vmcnt(10)" ::: "memory");
        if (u == 9) asm volatile("s_waitcnt vmcnt(14)" ::: "memory");
        if (u == 10) { asm volatile("s_waitcnt vmcnt(8)" ::: "memory"); lds_inc(Y_B); }
        if (u == 13) lds_wait<0>(Y_B, 8, budget);
        if (u == 17 || u == 21 || u == 25) asm volatile("s_waitcnt vmcnt(9)" ::: "memory");
        const bool xs = u < 2 || (u >= 6 && u < 14);
        SB();
        MFMA32(e.c[0][0], fa[u & 3], fb[u & 3][0]);
        SB();
        if (u + 3 < 32) { G1_A(u + 3); G1_B(u + 3, 0); }
        if (xs) { if (u & 1) dma_n<2048>(e.wsrd, e.wv[2], e.wso); else dma_m<0>(e.wsrd, e.wv[0], e.wso, e.wdst); }
        SB();
        MFMA32(e.c[0][1], fa[u & 3], fb[u & 3][1]);
        SB();
        if (u + 3 < 32) G1_B(u + 3, 1);
        if (xs) { if (u & 1) dma_n<3072>(e.wsrd, e.wv[3], e.wso); else dma_n<1024>(e.wsrd, e.wv[1], e.wso); }
        if (u >= 2 && u < 6) b_panel(u + 2);
        if (u == 14) { if (act3) set_w1(); else set_c(); }
        if (u >= 16) {
          if ((u & 3) == 0) dma_m<0>(e.wsrd, e.wv[0], e.wso, e.wdst);
          if ((u & 3) == 1) dma_n<1024>(e.wsrd, e.wv[1], e.wso);
          if ((u & 3) == 2) dma_n<2048>(e.wsrd, e.wv[2], e.wso);
          if ((u & 3) == 3) dma_n<3072>(e.wsrd, e.wv[3], e.wso);
        }
        if ((xs && (u & 1)) || (u >= 16 && (u & 3) == 3)) { e.wso += e.wstep; e.wdst = nxt(e, e.wdst); if (u >= 16 && !act3) c_wrap(); }
      }
      e.rsl = e.rlo + (8 % R) * 4096;
      STAMP(3);
      lds_inc(Y_G1);
      lds_wait<0>(Y_G1, 9, budget);
      const u32 a = L_DUMP + w * 8192 + lane * 16;
      dump16(e.c[0][0], a); dump16(e.c[0][1], a + 4096);
      lds_inc(Y_D1);
      STAMP(4);
    }
    auto set_hx = [&]() { e.xv = hh * 1024 + (16 * ((l31 >> 2) & 1) + 4 * (l31 >> 3) + (l31 & 3)) * 16; e.xso = OFF_HX + st * 131072 + w * 32768; e.xs1 = 4096; e.xs2 = 4096; };
    if (act3) {
      e.xv = hh * 1024 + l31 * 16; e.xso = OFF_HN + dt3 * 262144 + w * 4096; e.xs1 = 16384; e.xs2 = 16384;
      lds_wait(Y_GATE1, 1, budget);
      STAMP(5);
      x_prologue(e);
      group_first<true>(e);
#pragma unroll 1
      for (int g = 1; g < 7; g++) {
        if (g == 6) set_c();
        group<true, true>(e);
      }
      c_wrap();
      STAMP(6);
      set_hx();
      const u32 hso = e.xso;
      const int open2 = uni(*VLP(int, YA(Y_GATE2)));
      cfence();
      group_maybe<true>(e, open2);
      c_wrap();
      STAMP(7);
      lds_wait(Y_HS, 8, budget);
      dump_all(e, w, lane);
      lds_inc(Y_D3);
      STAMP(8);
      lds_wait(Y_GATE2, 1, budget);
      x_prologue_unless(e, hso, hso + 4096, open2);
      STAMP(14);
    } else {
      const int q = (idx - 22) * 4 + w;
      u32 sink = 0;
      auto touch = [&](u32x4 srd, u32 v, u32 so, u32 step, int n0, int n1) {
#pragma unroll 1
        for (int n = n0 + q; n < n1; n += 40)
          asm volatile("buffer_load_dword %0, %1, %2, %3 offen\n\ts_waitcnt vmcnt(3)" : "+v"(sink) : "v"(v), "s"(srd), "s"(so + n * step) : "memory");
      };
      if (PF & 1) touch(make_srd(p.w1, S * F * 2), rw * 2752 + min(pos * 128, 764u), ((22 * (xcd >> 1)) >> 2) * 128, 8 * 2752, 64, 256);
      if (PF & 2) { lds_wait<2>(Y_GATE1, 1, budget); touch(e.xsrd, (lane >> 4) * 262144 + (lane & 15) * 128, OFF_HN + (xcd & 1) * 1048576, 2048, 16, 128); }
      if (PF & 16) { asm volatile("s_waitcnt vmcnt(0)" : "+v"(sink) :: "memory"); lds_wait<4>(Y_H, 8, budget); }
      if (PF & 4) { touch(make_srd(p.Cre, D * P * 2), lane * 128, (xcd & 1) * 262144, 8192, 0, 32);
                    touch(make_srd(p.Cim, D * P * 2), lane * 128, (xcd & 1) * 262144, 8192, 0, 32); }
      if (PF & 8) touch(make_srd(p.w2, F * S * 2), rw * 4096 + pos * 128, (xcd >> 1) * 1024, 32768, 0, 172);
      asm volatile("s_waitcnt vmcnt(0)" : "+v"(sink) :: "memory");
      STAMP(8);
      lds_wait<4>(Y_GATE2, 1, budget);
      set_hx();
      x_prologue(e);
    }
    e.wsrd = make_srd(w < 2 ? p.Cre : p.Cim, D * P * 2);
    group_first<false>(e);
    c_wrap();
    group<false, true>(e);
    set_w2();
    group<false, true>(e);
    STAMP(9);
    e.xv = hh * 1024 + l31 * 16; e.xso = OFF_U + dt * 180224 + w * 4096; e.xs1 = 16384; e.xs2 = 16384;
    const u32 uso = e.xso;
    const int open3 = uni(*VLP(int, YA(Y_GATE3)));
    cfence();
    group_maybe<false>(e, open3);
    lds_wait(act3 ? Y_R3 : Y_HS, 8, budget);
    dump_all(e, w, lane);
    lds_inc(Y_D2);
    STAMP(10);
    lds_wait(Y_GATE3, 1, budget);
    x_prologue_unless(e, uso, uso + 16384, open3);
    STAMP(11);
    group_first<true>(e);
    group<true, true>(e);
    group<true, true>(e);
    unit<true, 0, true, 1, 12, true>(e); unit<true, 1, true, 1, 12, true>(e);
    unit<true, 2, false, 1, 12, true>(e); unit<true, 3, false, 1, 10, true>(e);
    unit<true, 0, false, 1, 8, true>(e); unit<true, 1, false, 1, 6, true>(e);
    unit<true, 2, false, 0, 6, true>(e); unit<true, 3, false, 0, 4, true>(e);
    unit<true, 0, false, 0, 2, true>(e); unit<true, 1, false, 0, 0, false>(e);
    STAMP(12);
    lds_wait(Y_R2, 8, budget);
    dump_all(e, w, lane);
    lds_inc(Y_D4);
    STAMP(13);
    DUMPT();
    return;
  }

  const int j = w - 4;
  const rsrc_t wsr = make_rsrc(p.ws, WS_BYTES);
  const u32x4 raw = *(const u32x4*)(p.x + (koct * 8 + j) * D + lane * 8);
  const u32 tag0 = __builtin_amdgcn_raw_buffer_load_b32(wsr, OFF_E, 0, SC1);
  u32 i0 = 0, i1 = 0, i2 = 0;
  if (j == 1) { i0 = p.Lre[32 * g1 + l31]; i1 = p.Lim[32 * g1 + l31]; i2 = p.ldt[32 * g1 + l31]; }
  if (j == 0) i0 = p.b2[64 * st + lane];
  asm volatile("s_barrier" ::: "memory");
  __builtin_amdgcn_s_setprio(1);
  {
    float v[8];
#pragma unroll
    for (int i = 0; i < 4; i++) { v[2 * i] = bf2f(raw[i] & 0xffff); v[2 * i + 1] = bfhi(raw[i]); }
    const float piv = __builtin_bit_cast(float, __builtin_amdgcn_readfirstlane(__builtin_bit_cast(int, v[0])));
#pragma unroll
    for (int i = 0; i < 8; i++) v[i] -= piv;
    float sum = ((v[0] + v[1]) + (v[2] + v[3])) + ((v[4] + v[5]) + (v[6] + v[7]));
    float q = ((v[0] * v[0] + v[1] * v[1]) + (v[2] * v[2] + v[3] * v[3])) + ((v[4] * v[4] + v[5] * v[5]) + (v[6] * v[6] + v[7] * v[7]));
    wave_sum2(sum, q);
    const float ms = sum * (1.f / D), var = fmaxf(q * (1.f / D) - ms * ms, 0.f);
    const float r = __builtin_amdgcn_rsqf(var + 1e-5f);
    const float rr = r * __builtin_amdgcn_rsqf(var * r * r + 1e-5f);
    if (lane == 0) *LP(f32x2, L_ST0 + j * 8) = f32x2{ms + piv, r};
    u32x4 o;
#pragma unroll
    for (int i = 0; i < 4; i++) o[i] = pk2((v[2 * i] - ms) * rr, (v[2 * i + 1] - ms) * rr);
    *LP(u32x4, L_LNT + j * 1088 + lane * 16) = o;
    lds_inc(Y_LN);
  }
  STAMP(1);
  const u32 tag = uni(tag0) + 1u;
  {
    lds_wait<0>(Y_LN, 8, budget);
    const u32 a = L_LNT + ((lane & 15) >> 2) * 1088 + j * 128 + (lane >> 4) * 32 + (lane & 3) * 8;
    const bf16x8 t = tr_pair(a, a + 4 * 1088);
    const f32x2 sv = *LP(f32x2, L_ST0 + (lane & 7) * 8);
    __builtin_amdgcn_raw_buffer_store_b128(__builtin_bit_cast(u32x4, t), wsr, OFF_HN + j * 262144 + koct * 1024 + lane * 16, 0, SC1);
    if (j == 2 && lane < 8) { const u32x4 gr = {__float_as_uint(sv[0]), tag, __float_as_uint(sv[1]), tag};
                              __builtin_amdgcn_raw_buffer_store_b128(gr, wsr, OFF_STAT + (koct * 8 + lane) * 16, 0, SC1); }
    STAMP(2);
  }
  float a8r = 0, a8i = 0, qr = 0, qi = 0;
  if (j == 1) {
    float cr, ci;
    asm volatile("" : "+v"(i0), "+v"(i1), "+v"(i2));
    chan_const(i0, i1, i2, a8r, a8i, cr, ci);
    *LP(f32x4, L_CC + l31 * 16) = f32x4{a8r, a8i, cr, ci};
  }
  drain();
  if (lds_inc_rtn(Y_A) == 7 && lane < 8) __builtin_amdgcn_raw_buffer_store_b32(tag, wsr, OFF_FA + lane * 8192 + (koct >> 2) * 128 + (koct & 3) * 4, 0, SC1);
  STAMP(3);
  if (j >= 4) {
    lds_wait(Y_B, 8, budget);
    float s0 = 0, s1 = 0;
    const bf16x2 one2 = {(__bf16)1.0f, (__bf16)1.0f};
#pragma unroll
    for (int k = 0; k < 8; k++) {
      const u32x4 t = *LP(u32x4, L_DUMP + k * 8192 + (j - 4) * 2048 + lane * 16), u = *LP(u32x4, L_DUMP + k * 8192 + (j - 4) * 2048 + 1024 + lane * 16);
#pragma unroll
      for (int i = 0; i < 4; i++) {
        const u32 ti = t[i], ui = u[i];
        s0 = __builtin_amdgcn_fdot2_f32_bf16(__builtin_bit_cast(bf16x2, ti), one2, s0, false);
        s1 = __builtin_amdgcn_fdot2_f32_bf16(__builtin_bit_cast(bf16x2, ui), one2, s1, false);
      }
    }
    s0 = dpp_add<0xB1, 0xf>(s0); s1 = dpp_add<0xB1, 0xf>(s1); s0 = dpp_add<0x4E, 0xf>(s0); s1 = dpp_add<0x4E, 0xf>(s1);
    s0 = dpp_add<0x141, 0xf>(s0); s1 = dpp_add<0x141, 0xf>(s1);
    if ((lane & 7) == 0) { *LP(float, L_BS + (16 * (j - 4) + (lane >> 3)) * 4) = s0; *LP(float, L_BS + (16 * (j - 4) + 8 + (lane >> 3)) * 4) = s1; }
    lds_inc(Y_G1);
  }
  if (j == 1) lds_inc(Y_G1);
  if (j == 1) {
    csq(a8r, a8i); csq(a8r, a8i); csq(a8r, a8i);
    qr = a8r; qi = a8i;
    csq(qr, qi); csq(qr, qi); csq(qr, qi);
    float tr = hh ? qr : 1.f, ti = hh ? qi : 0.f;
#pragma unroll
    for (int i = 0; i < 8; i++) { *LP(f32x2, L_PW + (8 * hh + i) * 256 + l31 * 8) = f32x2{tr, ti}; float t = a8r * tr - a8i * ti; ti = a8r * ti + a8i * tr; tr = t; }
  }
  if (j == 0) {
    lds_wait(Y_A, 8, budget);
    __builtin_amdgcn_s_sleep(D1);
    bool needA = act3 || (PF & 2), needS = true;
    for (;;) {
      u32x4 fa = {tag, tag, tag, tag}, a, c, d;
      if (needA) fa = __builtin_amdgcn_raw_buffer_load_b128(wsr, OFF_FA + xcd * 8192 + lane * 128, 0, SC1);
      if (needS) {
        a = __builtin_amdgcn_raw_buffer_load_b128(wsr, OFF_STAT + (128 * c1 + lane) * 16, 0, SC1);
        c = __builtin_amdgcn_raw_buffer_load_b128(wsr, OFF_STAT + (128 * c1 + 64 + lane) * 16, 0, SC1);
        d = __builtin_amdgcn_raw_buffer_load_b128(wsr, OFF_STAT + (64 * st + lane) * 16, 0, SC1);
      }
      const bool to = --budget < 0;
      if (needA && (__all(fa[0] == tag && fa[1] == tag && fa[2] == tag && fa[3] == tag) || to)) { lds_inc(Y_GATE1); needA = false; STAMP(14); }
      if (needS && (__all(a[1] == tag && a[3] == tag && c[1] == tag && c[3] == tag && d[1] == tag && d[3] == tag) || to)) {
        *LP(u32x2, L_ST1 + lane * 8) = u32x2{a[0], a[2]};
        *LP(u32x2, L_ST1 + 512 + lane * 8) = u32x2{c[0], c[2]};
        *LP(u32x4, L_ST2 + lane * 16) = u32x4{d[0], d[2], i0 << 16, 0u};
        lds_inc(Y_D1);
        needS = false;
      }
      if (!needA && !needS) break;
      __builtin_amdgcn_s_sleep(PSLEEP);
    }
    if (b == 0 && lane == 0) __builtin_amdgcn_raw_buffer_store_b32(tag, wsr, OFF_E, 0, SC1);
  }
  const int mi = j >> 2, ni = (j >> 1) & 1, rh = j & 1;
  const int rl = 32 * mi + 16 * hh + 8 * rh;
  const int f0 = 64 * ft + rl, s0 = 64 * st + rl, dd = 64 * dt + 32 * ni + l31;
  const rsrc_t outr = make_rsrc(p.out, S * D * 4);
  {
    lds_wait(Y_D1, 5, budget);
    STAMP(4);
    const int en = j >> 1, seg = 4 * en + 2 * hh + rh;
    const u32 da = L_DUMP + en * 8192 + rh * 2048 + lane * 16;
    const f32x4 pr0 = *LP(f32x4, da), pr1 = *LP(f32x4, da + 1024), pi0 = *LP(f32x4, da + 4096), pi1 = *LP(f32x4, da + 5120);
    const f32x4 cc = *LP(f32x4, L_CC + l31 * 16);
    const float bsr = *LP(float, L_BS + l31 * 4), bsi = *LP(float, L_BS + 128 + l31 * 4);
    const float ar = cc[0], ai = cc[1], cr = cc[2], ci = cc[3];
    float vr[8], vi[8], er = 0, ei = 0;
#pragma unroll
    for (int i2_ = 0; i2_ < 4; i2_++) {
      const f32x4 sv = *LP(f32x4, L_ST1 + seg * 64 + i2_ * 16);
#pragma unroll
      for (int u = 0; u < 2; u++) {
        const int i = 2 * i2_ + u;
        const float a_ = i < 4 ? pr0[i & 3] : pr1[i & 3], b_ = i < 4 ? pi0[i & 3] : pi1[i & 3];
        const float pr = (a_ - sv[2 * u] * bsr) * sv[2 * u + 1], pq = (b_ - sv[2 * u] * bsi) * sv[2 * u + 1];
        vr[i] = cr * pr - ci * pq; vi[i] = cr * pq + ci * pr;
        const float t = ar * er - ai * ei + vr[i]; ei = ar * ei + ai * er + vi[i]; er = t;
      }
    }
    *LP(f32x2, L_SEG + seg * 256 + l31 * 8) = f32x2{er, ei};
    lds_inc(Y_E);
    STAMP(5);
    if (j == 1) {
      float br = qr, bi = qi;
      csq(br, bi);
      float dr = br, di = bi;
      csq(dr, di);
      lds_wait<0>(Y_E, 8, budget);
      f32x2 ev[8], pf[8];
#pragma unroll
      for (int i = 0; i < 8; i++) ev[i] = *LP(f32x2, L_SEG + (8 * hh + i) * 256 + l31 * 8);
      float gr_ = 0, gi_ = 0;
#pragma unroll
      for (int i = 0; i < 8; i++) { pf[i] = f32x2{gr_, gi_}; float t = a8r * gr_ - a8i * gi_ + ev[i][0]; gi_ = a8r * gi_ + a8i * gr_ + ev[i][1]; gr_ = t; }
      auto sw0 = __builtin_amdgcn_permlane32_swap(__float_as_uint(gr_), __float_as_uint(gr_), false, false);
      auto sw1 = __builtin_amdgcn_permlane32_swap(__float_as_uint(gi_), __float_as_uint(gi_), false, false);
      float lr_ = hh ? __uint_as_float(sw0[0]) : 0.f, li_ = hh ? __uint_as_float(sw1[0]) : 0.f;
      if (hh) { const u32x4 g4 = {__float_as_uint(gr_ + qr * lr_ - qi * li_), tag, __float_as_uint(gi_ + qr * li_ + qi * lr_), tag};
                __builtin_amdgcn_raw_buffer_store_b128(g4, wsr, OFF_AGG + (c1 * 512 + 32 * g1 + l31) * 16, 0, SC1); }
      STAMP(14);
#pragma unroll
      for (int i = 0; i < 8; i++) {
        *LP(f32x2, L_CIN + (8 * hh + i) * 256 + l31 * 8) = f32x2{pf[i][0] + lr_, pf[i][1] + li_};
        float t = a8r * lr_ - a8i * li_; li_ = a8r * li_ + a8i * lr_; lr_ = t;
      }
      __builtin_amdgcn_s_sleep(D2);
      float hr = 0, hi = 0;
      for (; !(ABL & 1);) {
        u32x4 a[8]; bool ok = true;
#pragma unroll
        for (int i = 0; i < 8; i++)
          a[i] = __builtin_amdgcn_raw_buffer_load_b128(wsr, 2 * i + hh < c1 ? OFF_AGG + ((2 * i + hh) * 512 + 32 * g1 + l31) * 16 : WS_BYTES, 0, SC1);
        hr = 0; hi = 0;
#pragma unroll
        for (int i = 0; i < 8; i++) {
          const bool use = 2 * i + hh < c1;
          ok = ok && (!use || (a[i][1] == tag && a[i][3] == tag));
          float t = dr * hr - di * hi + __uint_as_float(a[i][0]), u = dr * hi + di * hr + __uint_as_float(a[i][2]);
          hr = use ? t : hr; hi = use ? u : hi;
        }
        if (__all(ok) || --budget < 0) break;
        __builtin_amdgcn_s_sleep(PSLEEP);
      }
      if (((c1 - 1) & 1) != hh) { float t = br * hr - bi * hi; hi = br * hi + bi * hr; hr = t; }
      auto s0_ = __builtin_amdgcn_permlane32_swap(__float_as_uint(hr), __float_as_uint(hr), false, false);
      auto s1_ = __builtin_amdgcn_permlane32_swap(__float_as_uint(hi), __float_as_uint(hi), false, false);
      *LP(f32x2, L_H + l31 * 8) = f32x2{__uint_as_float(s0_[0]) + __uint_as_float(s0_[1]), __uint_as_float(s1_[0]) + __uint_as_float(s1_[1])};
      lds_inc(Y_CIN);
    }
    lds_wait<2>(Y_CIN, 1, budget);
    STAMP(6);
    const f32x2 cv = *LP(f32x2, L_CIN + seg * 256 + l31 * 8), pw = *LP(f32x2, L_PW + seg * 256 + l31 * 8), hv = *LP(f32x2, L_H + l31 * 8);
    float hr = cv[0] + pw[0] * hv[0] - pw[1] * hv[1], hi = cv[1] + pw[0] * hv[1] + pw[1] * hv[0];
#pragma unroll
    for (int i = 0; i < 8; i++) { float t = ar * hr - ai * hi + vr[i]; hi = ar * hi + ai * hr + vi[i]; hr = t; vr[i] = hr; vi[i] = -hi; }
    u32x4 o0, o1;
#pragma unroll
    for (int i = 0; i < 4; i++) { o0[i] = pk2(vr[2 * i], vr[2 * i + 1]); o1[i] = pk2(vi[2 * i], vi[2 * i + 1]); }
    const u32 sc = L_DUMP + 32768 + j * 2048;
    *LP(u32x4, sc + l31 * 32 + hh * 16) = o0;
    *LP(u32x4, sc + 1024 + l31 * 32 + hh * 16) = o1;
    cfence();
    const u32 ta = sc + (8 * (lane >> 4) + ((lane & 15) >> 2)) * 32 + (lane & 3) * 8;
    const bf16x8 t0 = tr_pair(ta, ta + 128), t1 = tr_pair(ta + 1024, ta + 1024 + 128);
    const int tk = 32 * (en & 1) + 16 * ((lane >> 3) & 1) + 8 * rh + (lane & 7);
    const u32 off = OFF_HX + (2 * c1 + (en >> 1)) * 131072 + (4 * g1 + (lane >> 4)) * 1024 + tk * 16;
    if (!(ABL & 2)) {
    __builtin_amdgcn_raw_buffer_store_b128(__builtin_bit_cast(u32x4, t0), wsr, off, 0, SC1);
    __builtin_amdgcn_raw_buffer_store_b128(__builtin_bit_cast(u32x4, t1), wsr, off, 65536, SC1); } else asm volatile("" :: "v"(t0), "v"(t1));
    lds_inc(Y_HS);
    drain();
    const u32x2 f2 = {tag, tag};
    if (lds_inc_rtn(Y_H) == 7 && lane == 0) __builtin_amdgcn_raw_buffer_store_b64(f2, wsr, OFF_FH + (c1 * 16 + g1) * 128, 0, SC1);
    STAMP(7);
  }
  u32x4 b1v = {0, 0, 0, 0};
  if (act3 && f0 < F) b1v = *(const u32x4*)(p.b1 + f0);
  const float dsk = bf2f(p.Dsk[dd]);
  {
    const u32x4 xt = *(const u32x4*)(p.x + (64 * st + 8 * j + (lane >> 3)) * D + 64 * dt + (lane & 7) * 8);
    lds_wait(Y_HS, 8, budget);
    *LP(u32x4, L_LNT + j * 1024 + lane * 16) = xt;
    lds_inc(Y_D2);
  }
  if (j == 0) {
    __builtin_amdgcn_s_sleep(DH);
    if (ABL & 4) { if (!act3) for (int i = 0; i < 3; i++) __builtin_amdgcn_s_sleep(64); }
    for (; !(ABL & 4);) {
      u32x2 a = __builtin_amdgcn_raw_buffer_load_b64(wsr, OFF_FH + (st >> 1) * 2048 + (lane & 15) * 128, 0, SC1);
      if (__all(a[0] == tag && a[1] == tag) || --budget < 0) break;
      __builtin_amdgcn_s_sleep(2 * PSLEEP);
    }
    lds_inc(Y_GATE2);
    STAMP(15);
  }
  if (act3) {
    lds_wait(Y_D3, 4, budget);
    STAMP(8);
    float o[8];
    gather(o, j, lane);
    lds_inc(Y_R3);
#pragma unroll
    for (int i = 0; i < 8; i++) {
      const float a = o[i] + ((i & 1) ? bfhi(b1v[i >> 1]) : bf2f(b1v[i >> 1] & 0xffff));
      const float z = -2.302208198f * (a + 0.044715f * a * a * a);
      o[i] = a * __builtin_amdgcn_rcpf(1.f + __builtin_amdgcn_exp2f(z));
    }
    u32x4 uo;
#pragma unroll
    for (int i = 0; i < 4; i++) uo[i] = f0 < F ? pk2(o[2 * i], o[2 * i + 1]) : 0u;
    __builtin_amdgcn_raw_buffer_store_b128(uo, wsr, OFF_U + dt3 * 180224 + (f0 >> 3) * 1024 + (32 * ni + l31) * 16, 0, SC1);
    drain();
    const u32x2 f2 = {tag, tag};
    if (lds_inc_rtn(Y_U) == 7 && lane < 4) __builtin_amdgcn_raw_buffer_store_b64(f2, wsr, OFF_FU + lane * 32768 + dt3 * 4096 + ft * 128, 0, SC1);
    STAMP(9);
  }
  if (j == 0) {
    if (act3) __builtin_amdgcn_s_sleep(D3); else __builtin_amdgcn_s_sleep(100);
    for (;;) {
      u32x2 a = __builtin_amdgcn_raw_buffer_load_b64(wsr, OFF_FU + (xcd >> 1) * 32768 + dt * 4096 + min(lane, 21) * 128, 0, SC1);
      if (__all(a[0] == tag && a[1] == tag) || --budget < 0) break;
      __builtin_amdgcn_s_sleep(PSLEEP);
    }
    lds_inc(Y_GATE3);
    STAMP(10);
  }
  float sp[8], b2v[8], xv[8];
  lds_wait<2>(Y_D2, 12, budget);
  gather(sp, j, lane);
  lds_inc(Y_R2);
#pragma unroll
  for (int i = 0; i < 8; i++) {
    const f32x4 sv = *LP(f32x4, L_ST2 + (rl + i) * 16);
    xv[i] = bf2f(*LP(u16, L_LNT + (rl + i) * 128 + (32 * ni + l31) * 2));
    sp[i] += (xv[i] - sv[0]) * sv[1] * dsk; b2v[i] = sv[2];
    asm volatile("" : "+v"(sp[i]), "+v"(b2v[i]), "+v"(xv[i]));
  }
  STAMP(11);
  lds_wait(Y_D4, 4, budget);
  STAMP(12);
  {
    float o[8];
    gather(o, j, lane);
#pragma unroll
    for (int i = 0; i < 8; i++) {
      const float gv = o[i] + b2v[i];
      const float sg = gv * __builtin_amdgcn_rcpf(1.f + __builtin_amdgcn_exp2f(-1.442695041f * gv));
      __builtin_amdgcn_raw_buffer_store_b32(__float_as_uint(xv[i] + sp[i] * sg), outr, (s0 * D + dd) * 4, i * 2048, OUTAUX);
    }
  }
  STAMP(13);
  DUMPT();
}

#if defined(__HIP_DEVICE_COMPILE__) && __HIP_DEVICE_COMPILE__
#pragma clang attribute pop
#endif

extern "C" void kernel_launch(void* const* d_in, const int* in_sizes, int n_in, void* d_out, int out_size, void* d_ws,
                              size_t ws_size, hipStream_t stream) {
  Params p;
  p.x = (const u16*)d_in[0]; p.Lre = (const u16*)d_in[1]; p.Lim = (const u16*)d_in[2];
  p.Bre = (const u16*)d_in[3]; p.Bim = (const u16*)d_in[4]; p.Cre = (const u16*)d_in[5]; p.Cim = (const u16*)d_in[6];
  p.ldt = (const u16*)d_in[7]; p.Dsk = (const u16*)d_in[8];
  p.w1 = (const u16*)d_in[10]; p.b1 = (const u16*)d_in[11]; p.w2 = (const u16*)d_in[12]; p.b2 = (const u16*)d_in[13];
  p.out = (float*)d_out; p.ws = (char*)d_ws;
  (void)hipFuncSetAttribute((const void*)s5_mixer_kernel, hipFuncAttributeMaxDynamicSharedMemorySize, LDS_BYTES);
  s5_mixer_kernel<<<256, NWAVE * 64, LDS_BYTES, stream>>>(p);
}
```
